```python
import torch

def _softmax(x, axis=-1):
    return torch.softmax(x, dim=axis)
def _gelu(x):
    return torch.nn.functional.gelu(x, approximate="tanh")
def _silu(x):
    return torch.nn.functional.silu(x)
def _sigmoid(x):
    return torch.sigmoid(x)
def _layernorm(x, axis=-1, eps=1e-5):
    m = x.mean(dim=axis, keepdim=True)
    v = x.var(dim=axis, keepdim=True, unbiased=False)
    return (x - m) / torch.sqrt(v + eps)
def _scatter_add(tgt, idx, src, axis=0):
    return tgt.clone().index_add_(axis, idx, src)
def _segment_sum(src, seg, n):
    out = torch.zeros((n,) + src.shape[1:], dtype=src.dtype)
    return out.index_add_(0, seg, src)
def _pad(x, pw):
    flat = tuple(v for pair in reversed(pw) for v in pair)
    return torch.nn.functional.pad(x, flat)

kernel_name = 'standalone__mha_softmax__near_ridge'

def setup_inputs():

    _w = (lambda a: (lambda t: t.to(torch.bfloat16) if t.is_floating_point() else t)(torch.as_tensor(a)))
    B, H, Sq, Sk, D = 8, 32, 256, 512, 128
    return {
        "q": _w((lambda *s: torch.randn(*s, dtype=torch.bfloat16) * 0.8)(B, H, Sq, D)),
        "k": _w((lambda *s: torch.randn(*s, dtype=torch.bfloat16) * 0.8)(B, H, Sk, D)),
        "v": _w((lambda *s: torch.randn(*s, dtype=torch.bfloat16) * 0.05)(B, H, Sk, D)),
    }

def reference(q, k, v):
    scale = q.shape[-1] ** -0.5
    scores = torch.einsum("bhqd,bhkd->bhqk", q, k) * scale
    p = _softmax(scores, axis=-1)
    return torch.einsum("bhqk,bhkd->bhqd", p, v)
```

```cpp
#include <hip/hip_runtime.h>
#include <hip/hip_bf16.h>
#include <cstdio>

using bf16 = __hip_bfloat16;
constexpr int   D = 128, NW = 8, QBLK = 32, KVBLK = 64;
constexpr float SCALE = 0.088388347648318440f;
#define ATTN_THR 8.f
constexpr float THR = ATTN_THR;
#define ATTN_LAYOUT 0
constexpr int LAYOUT = ATTN_LAYOUT;
#define ATTN_SDEPTH 2
constexpr int SDEPTH = ATTN_SDEPTH;
static_assert(SDEPTH == 1 || SDEPTH == 2, "SDEPTH is 1 or 2");
#define ATTN_B 8
#define ATTN_HQ 32
#define ATTN_HKV 32
#define ATTN_S 256
#define ATTN_SKV 512
constexpr int B = ATTN_B, HQ = ATTN_HQ, HKV = ATTN_HKV, S = ATTN_S, SKV = ATTN_SKV;
constexpr int LDQ = LAYOUT ? HQ * D : D, LDK = LAYOUT ? HKV * D : D, LDO = LDQ;
static_assert(S % (QBLK * NW) == 0 && SKV % (2 * KVBLK) == 0 && HQ % HKV == 0, "S % 256 == 0, SKV % 128 == 0 (even tile count), HQ % HKV == 0");
constexpr size_t SHM_V = KVBLK * D * 2, SHM_K = KVBLK * D * 2, SHM_ATTN = 2 * SHM_V + 2 * SHM_K + NW * 64 * 4;

using bf16x8 = __attribute__((ext_vector_type(8))) short;
using s16x4  = __attribute__((ext_vector_type(4))) short;
using f32x16 = __attribute__((ext_vector_type(16))) float;
using f32x8  = __attribute__((ext_vector_type(8))) float;
using u32x4  = __attribute__((ext_vector_type(4))) unsigned;
#define KSWZ(row, colB) ((row) * 256 + ((colB) ^ (((row) & 7) << 4)))
#define SBAR() __builtin_amdgcn_sched_barrier(0)
__device__ __forceinline__ int crow(int r, int hi) { return (r & 3) + 8 * (r >> 2) + 4 * hi; }
__device__ __forceinline__ unsigned cvtpk(float lo, float hi) {
  unsigned r; asm volatile("v_cvt_pk_bf16_f32 %0, %1, %2" : "=v"(r) : "v"(lo), "v"(hi)); return r;
}
template <typename TIn> struct Stage;
template <> struct Stage<bf16>  { using T = bf16x8;
  __device__ static __forceinline__ T ld8(const bf16* p) { return *reinterpret_cast<const bf16x8*>(p); }
  __device__ static __forceinline__ bf16x8 tobf(T x) { return x; } };
template <> struct Stage<float> { using T = f32x8;
  __device__ static __forceinline__ T ld8(const float* p) { return *reinterpret_cast<const f32x8*>(p); }
  __device__ static __forceinline__ bf16x8 tobf(T x) {
    u32x4 w = {cvtpk(x[0], x[1]), cvtpk(x[2], x[3]), cvtpk(x[4], x[5]), cvtpk(x[6], x[7])}; return *reinterpret_cast<bf16x8*>(&w); } };

__device__ __forceinline__ void partialSM(f32x16& p0, f32x16& p1, float& m_reg, float& mn, float& alpha) {
  constexpr float C = SCALE * 1.4426950408889634f;
  float pmax = p0[0]; for (int r = 1; r < 16; ++r) pmax = fmaxf(pmax, p0[r]); for (int r = 0; r < 16; ++r) pmax = fmaxf(pmax, p1[r]);
  { auto rr = __builtin_amdgcn_permlane32_swap(__float_as_uint(pmax), __float_as_uint(pmax), false, false);
    pmax = fmaxf(__uint_as_float(rr[0]), __uint_as_float(rr[1])); }
  if (__builtin_expect(__all(pmax - m_reg <= THR / SCALE), 1)) { mn = m_reg; alpha = 1.f; }
  else { mn = fmaxf(m_reg, pmax); alpha = __builtin_amdgcn_exp2f((m_reg - mn) * C); m_reg = mn; }
  float mnC = -mn * C;
  for (int r = 0; r < 16; ++r) p0[r] = fmaf(p0[r], C, mnC); for (int r = 0; r < 16; ++r) p1[r] = fmaf(p1[r], C, mnC);
  for (int r = 0; r < 16; ++r) p0[r] = __builtin_amdgcn_exp2f(p0[r]);
}
__device__ __forceinline__ void finishSM(f32x16& p0, f32x16& p1, float alpha, float& l_reg, bf16x8& pa0, bf16x8& pa1, bf16x8& pa2, bf16x8& pa3) {
  for (int r = 0; r < 16; ++r) p1[r] = __builtin_amdgcn_exp2f(p1[r]);
  float ps = 0; for (int r = 0; r < 16; ++r) ps += p0[r]; for (int r = 0; r < 16; ++r) ps += p1[r];
  { auto rr = __builtin_amdgcn_permlane32_swap(__float_as_uint(ps), __float_as_uint(ps), false, false);
    ps = __uint_as_float(rr[0]) + __uint_as_float(rr[1]); }
  l_reg = l_reg * alpha + ps;
#define PK4(P, BASE, OUT) do { unsigned a0 = cvtpk(P[BASE + 0], P[BASE + 1]), a1 = cvtpk(P[BASE + 2], P[BASE + 3]);   \
    unsigned b0 = cvtpk(P[BASE + 4], P[BASE + 5]), b1 = cvtpk(P[BASE + 6], P[BASE + 7]);                              \
    auto r0 = __builtin_amdgcn_permlane32_swap(a0, b0, false, false); auto r1 = __builtin_amdgcn_permlane32_swap(a1, b1, false, false); \
    u32x4 w = {r0[0], r1[0], r0[1], r1[1]}; OUT = *reinterpret_cast<bf16x8*>(&w); } while (0)
  PK4(p0, 0, pa0); PK4(p0, 8, pa1); PK4(p1, 0, pa2); PK4(p1, 8, pa3);
#undef PK4
}
__device__ __forceinline__ void qkt(f32x16& p0, f32x16& p1, const bf16* Ks, const bf16x8* qr, int r32, int hi) {
  p0 = f32x16{}; p1 = f32x16{};
  for (int d0 = 0; d0 < 8; ++d0) { int cb = (d0 * 16 + hi * 8) * 2;
    bf16x8 b0 = *reinterpret_cast<const bf16x8*>((const char*)Ks + KSWZ(r32, cb));
    bf16x8 b1 = *reinterpret_cast<const bf16x8*>((const char*)Ks + KSWZ(32 + r32, cb));
    p0 = __builtin_amdgcn_mfma_f32_32x32x16_bf16(b0, qr[d0], p0, 0, 0, 0);
    p1 = __builtin_amdgcn_mfma_f32_32x32x16_bf16(b1, qr[d0], p1, 0, 0, 0); }
}
__device__ __forceinline__ int v_st(int k, int c) { const int kk = (k & ~0xC) | ((k & 4) << 1) | ((k & 8) >> 1); return ((kk >> 3) * 4 + (c >> 5)) * 512 + ((kk & 7) * 32 + (c & 31)) * 2; }
__device__ __forceinline__ int v_rd_base(int lane) { return ((lane & 3) << 3) | (((lane >> 2) & 3) << 6) | (((lane >> 4) & 1) << 5) | (((lane >> 5) & 1) << 8); }
constexpr int v_rd_off(int d0, int ks, int half) { return d0 * 512 + ks * 4096 + half * 2048; }
template <int OFF> __device__ __forceinline__ s16x4 tr_read(int vb) {
  s16x4 r; asm volatile("ds_read_b64_tr_b16 %0, %1 offset:%2" : "=&v"(r) : "v"(vb), "i"(OFF) : "memory"); return r;
}
template <int D0> __device__ __forceinline__ void pv_one(f32x16& od, int vb, bf16x8 pa0, bf16x8 pa1, bf16x8 pa2, bf16x8 pa3) {
  const s16x4 l0 = tr_read<v_rd_off(D0, 0, 0)>(vb), h0 = tr_read<v_rd_off(D0, 0, 1)>(vb), l1 = tr_read<v_rd_off(D0, 1, 0)>(vb), h1 = tr_read<v_rd_off(D0, 1, 1)>(vb);
  const s16x4 l2 = tr_read<v_rd_off(D0, 2, 0)>(vb), h2 = tr_read<v_rd_off(D0, 2, 1)>(vb), l3 = tr_read<v_rd_off(D0, 3, 0)>(vb), h3 = tr_read<v_rd_off(D0, 3, 1)>(vb);
  asm volatile("s_waitcnt lgkmcnt(0)" ::: "memory"); SBAR();
#define PK(L, H) (bf16x8){L[0], L[1], L[2], L[3], H[0], H[1], H[2], H[3]}
  od = __builtin_amdgcn_mfma_f32_32x32x16_bf16(pa0, PK(l0, h0), od, 0, 0, 0);
  od = __builtin_amdgcn_mfma_f32_32x32x16_bf16(pa1, PK(l1, h1), od, 0, 0, 0);
  od = __builtin_amdgcn_mfma_f32_32x32x16_bf16(pa2, PK(l2, h2), od, 0, 0, 0);
  od = __builtin_amdgcn_mfma_f32_32x32x16_bf16(pa3, PK(l3, h3), od, 0, 0, 0);
#undef PK
}
__device__ __forceinline__ void pv_d0(f32x16* o, int vb, bf16x8 pa0, bf16x8 pa1, bf16x8 pa2, bf16x8 pa3) {
  pv_one<0>(o[0], vb, pa0, pa1, pa2, pa3); pv_one<1>(o[1], vb, pa0, pa1, pa2, pa3); pv_one<2>(o[2], vb, pa0, pa1, pa2, pa3); pv_one<3>(o[3], vb, pa0, pa1, pa2, pa3);
}

template <typename TQ>
__device__ __forceinline__ void attn_dense_body(const TQ* __restrict__ Qb, const bf16* __restrict__ Kh, const bf16* __restrict__ Vh,
                                                bf16* __restrict__ Ob, int seq, char* lds) {
  using St = Stage<bf16>; using SQ = Stage<TQ>;
  const int tid = threadIdx.x, wid = tid >> 6, lane = tid & 63, r32 = lane & 31, hi = lane >> 5;
  bf16* V_lds = (bf16*)lds; bf16* K_lds = (bf16*)(lds + 2 * SHM_V);
  float* ws = (float*)(lds + 2 * SHM_V + 2 * SHM_K) + wid * 64; float* li_l = ws; float* al_l = ws + 32;
  float m_reg = -1e30f, l_reg = 0; f32x16 o[4] = {}; bf16x8 qr[8];
  const TQ* Qw = Qb + (long)(wid * QBLK + r32) * LDQ + hi * 8;
#pragma unroll
  for (int d0 = 0; d0 < 8; ++d0) qr[d0] = SQ::tobf(SQ::ld8(Qw + d0 * 16));
  const int sr = tid >> 4, sc = (tid & 15) * 8, vst0 = v_st(sr, sc), vst1 = v_st(32 + sr, sc);
  const int vb0 = (int)(uintptr_t)V_lds + v_rd_base(lane);
  struct { typename St::T vs0, vs1, ks0, ks1; } sr_[SDEPTH];
#define SLOAD(i, k0) do { sr_[i].vs0 = St::ld8(&Vh[(long)((k0) + sr) * LDK + sc]); sr_[i].vs1 = St::ld8(&Vh[(long)((k0) + 32 + sr) * LDK + sc]); \
    sr_[i].ks0 = St::ld8(&Kh[(long)((k0) + sr) * LDK + sc]); sr_[i].ks1 = St::ld8(&Kh[(long)((k0) + 32 + sr) * LDK + sc]); } while (0)
#define SWRITE(b, i) do { *(bf16x8*)((char*)V_lds + (b) * SHM_V + vst0) = St::tobf(sr_[i].vs0);          \
    *(bf16x8*)((char*)V_lds + (b) * SHM_V + vst1) = St::tobf(sr_[i].vs1); int kc = sc * 2;               \
    *(bf16x8*)((char*)K_lds + (b) * SHM_K + KSWZ(sr, kc)) = St::tobf(sr_[i].ks0);                       \
    *(bf16x8*)((char*)K_lds + (b) * SHM_K + KSWZ(32 + sr, kc)) = St::tobf(sr_[i].ks1); } while (0)
#define SWAIT() do { if constexpr (SDEPTH == 2) asm volatile("s_waitcnt vmcnt(4)" ::: "memory"); else asm volatile("s_waitcnt vmcnt(0)" ::: "memory"); } while (0)
#define RESC(a) do { if (__any((a) < 1.f)) { if (hi == 0) al_l[r32] = (a); asm volatile("s_waitcnt lgkmcnt(0)" ::: "memory"); \
    for (int d = 0; d < 4; ++d) for (int r = 0; r < 16; ++r) o[d][r] *= al_l[crow(r, hi)]; } } while (0)
  f32x16 pA0, pA1, pB0, pB1; float mnA, mnB, alA, alB; bf16x8 pa0, pa1, pa2, pa3; const int NT = seq / KVBLK;
  constexpr int SE = 0, SO = SDEPTH - 1;
  SLOAD(SE, 0); asm volatile("s_waitcnt vmcnt(0)" ::: "memory"); SWRITE(0, SE); __syncthreads();
  qkt(pA0, pA1, K_lds, qr, r32, hi); partialSM(pA0, pA1, m_reg, mnA, alA);
  SLOAD(SO, KVBLK); if constexpr (SDEPTH == 2) { if (2 < NT) SLOAD(SE, 2 * KVBLK); }
  SWAIT(); SWRITE(1, SO); __syncthreads();
  for (int j = 1; j + 1 < NT; j += 2) {
    SBAR(); qkt(pB0, pB1, (bf16*)((char*)K_lds + SHM_K), qr, r32, hi);
    finishSM(pA0, pA1, alA, l_reg, pa0, pa1, pa2, pa3); SBAR();
    SLOAD(SO, (j + SDEPTH) * KVBLK); SBAR();
    pv_d0(o, vb0, pa0, pa1, pa2, pa3); partialSM(pB0, pB1, m_reg, mnB, alB);
    __syncthreads(); SWAIT(); SWRITE(0, SE);
    RESC(alB); __syncthreads();
    SBAR(); qkt(pA0, pA1, K_lds, qr, r32, hi);
    finishSM(pB0, pB1, alB, l_reg, pa0, pa1, pa2, pa3); SBAR();
    if (SDEPTH == 1 || j + 3 < NT) SLOAD(SE, (j + 1 + SDEPTH) * KVBLK); SBAR();
    pv_d0(o, vb0 + (int)SHM_V, pa0, pa1, pa2, pa3); partialSM(pA0, pA1, m_reg, mnA, alA);
    __syncthreads(); SWAIT(); SWRITE(1, SO);
    RESC(alA); __syncthreads();
  }
  SBAR(); qkt(pB0, pB1, (bf16*)((char*)K_lds + SHM_K), qr, r32, hi);
  finishSM(pA0, pA1, alA, l_reg, pa0, pa1, pa2, pa3); SBAR();
  pv_d0(o, vb0, pa0, pa1, pa2, pa3); partialSM(pB0, pB1, m_reg, mnB, alB);
  __syncthreads(); RESC(alB);
  finishSM(pB0, pB1, alB, l_reg, pa0, pa1, pa2, pa3); SBAR();
  pv_d0(o, vb0 + (int)SHM_V, pa0, pa1, pa2, pa3);
  if (hi == 0) li_l[r32] = l_reg; asm volatile("s_waitcnt lgkmcnt(0)" ::: "memory");
  float rli[16];
#pragma unroll
  for (int r = 0; r < 16; ++r) rli[r] = __builtin_amdgcn_rcpf(li_l[crow(r, hi)]);
  bf16* Ow = Ob + (long)(wid * QBLK) * LDO;
#pragma unroll
  for (int r = 0; r < 16; ++r) { int orow = crow(r, hi);
    for (int d0 = 0; d0 < 4; ++d0) Ow[(long)orow * LDO + d0 * 32 + r32] = __float2bfloat16(o[d0][r] * rli[r]); }
#undef SLOAD
#undef SWRITE
#undef SWAIT
#undef RESC
}

template <typename TQ>
__global__ __launch_bounds__(64 * NW, 1)
void attn_fwd_dense(const TQ* __restrict__ Q, const bf16* __restrict__ K, const bf16* __restrict__ V, bf16* __restrict__ O, int sq, int skv, int GQA) {
  extern __shared__ __attribute__((aligned(16))) char shm[];
  const long b = blockIdx.z, h = blockIdx.y, Hq = gridDim.y, Hkv = Hq / GQA, kvh = h / GQA, i0 = (long)blockIdx.x * (QBLK * NW);
  if constexpr (LAYOUT == 0) {
    const long q0 = ((b * Hq + h) * sq + i0) * D, k0 = (b * Hkv + kvh) * skv * (long)D;
    attn_dense_body<TQ>(Q + q0, K + k0, V + k0, O + q0, skv, shm);
  } else {
    const long q0 = ((b * sq + i0) * HQ + h) * (long)D, k0 = (b * skv * HKV + kvh) * (long)D;
    attn_dense_body<TQ>(Q + q0, K + k0, V + k0, O + q0, skv, shm);
  }
}
__global__ __launch_bounds__(256) void cvt_f32_bf16(const float* __restrict__ a, bf16* __restrict__ ao, long na8,
                                                   const float* __restrict__ b, bf16* __restrict__ bo, long nb8) {
  for (long i = blockIdx.x * 256L + threadIdx.x; i < na8 + nb8; i += gridDim.x * 256L) {
    const bool inA = i < na8; const long j = inA ? i : i - na8;
    const f32x8 x = *reinterpret_cast<const f32x8*>((inA ? a : b) + j * 8);
    *reinterpret_cast<bf16x8*>((inA ? ao : bo) + j * 8) = Stage<float>::tobf(x);
  }
}

extern "C" void kernel_launch(void* const* d_in, const int* in_sizes, int n_in, void* d_out, int out_size, void* d_ws, size_t ws_size, hipStream_t stream) {
  hipFuncSetAttribute((const void*)attn_fwd_dense<bf16>, hipFuncAttributeMaxDynamicSharedMemorySize, (int)SHM_ATTN);
  hipLaunchKernelGGL(attn_fwd_dense<bf16>, dim3(S / (QBLK * NW), HQ, B), dim3(64 * NW), SHM_ATTN, stream,
                     (const bf16*)d_in[0], (const bf16*)d_in[1], (const bf16*)d_in[2], (bf16*)d_out, S, SKV, HQ / HKV);
}
```

```cpp
#include <hip/hip_runtime.h>
#include <hip/hip_bf16.h>

typedef unsigned u32x4 __attribute__((ext_vector_type(4)));

__device__ __forceinline__ float bf_lo(unsigned x) { return __uint_as_float(x << 16); }
__device__ __forceinline__ float bf_hi(unsigned x) { return __uint_as_float(x & 0xffff0000u); }

__device__ __forceinline__ void slow_rows(const char* __restrict__ Q, const char* __restrict__ K, const char* __restrict__ V, char* __restrict__ O,
                                       unsigned head, unsigned wid) {
  unsigned lane;
  asm volatile("v_mbcnt_lo_u32_b32 %0, -1, 0\n\tv_mbcnt_hi_u32_b32 %0, -1, %0" : "=v"(lane));
  const unsigned row = 64 * (lane >> 4) + 16 * wid + (lane & 15);
  const u32x4* qp = (const u32x4*)(Q + ((size_t)head * 256 + row) * 256);
  const u32x4* kp = (const u32x4*)(K + (size_t)head * 131072);
  const u32x4* vp = (const u32x4*)(V + (size_t)head * 131072);
  const float C = 0.12751743082f;
  float m = -INFINITY;
  for (int j = 0; j < 512; ++j) {
    float s = 0.f;
    for (int c = 0; c < 16; ++c) {
      const u32x4 a = qp[c], b = kp[j * 16 + c];
      for (int e = 0; e < 4; ++e) { s = fmaf(bf_lo(a[e]), bf_lo(b[e]), s); s = fmaf(bf_hi(a[e]), bf_hi(b[e]), s); }
    }
    m = fmaxf(m, s * C);
  }
  for (int c0 = 0; c0 < 16; c0 += 4) {
    float o[32];
#pragma unroll
    for (int d = 0; d < 32; ++d) o[d] = 0.f;
    float l = 0.f;
    for (int j = 0; j < 512; ++j) {
      float s = 0.f;
      for (int c = 0; c < 16; ++c) {
        const u32x4 a = qp[c], b = kp[j * 16 + c];
        for (int e = 0; e < 4; ++e) { s = fmaf(bf_lo(a[e]), bf_lo(b[e]), s); s = fmaf(bf_hi(a[e]), bf_hi(b[e]), s); }
      }
      const float p = __builtin_amdgcn_exp2f(s * C - m);
      l += p;
#pragma unroll
      for (int c = 0; c < 4; ++c) {
        const u32x4 b = vp[j * 16 + c0 + c];
#pragma unroll
        for (int e = 0; e < 4; ++e) { o[8 * c + 2 * e] = fmaf(p, bf_lo(b[e]), o[8 * c + 2 * e]); o[8 * c + 2 * e + 1] = fmaf(p, bf_hi(b[e]), o[8 * c + 2 * e + 1]); }
      }
    }
    const float inv = 1.f / l;
    u32x4* op = (u32x4*)(O + ((size_t)head * 256 + row) * 256);
#pragma unroll
    for (int c = 0; c < 4; ++c) {
      u32x4 w;
#pragma unroll
      for (int e = 0; e < 4; ++e) {
        const __hip_bfloat162 t = __float22bfloat162_rn(make_float2(o[8 * c + 2 * e] * inv, o[8 * c + 2 * e + 1] * inv));
        w[e] = *reinterpret_cast<const unsigned*>(&t);
      }
      op[c0 + c] = w;
    }
  }
}

__global__ __launch_bounds__(256, 1) void attn_fwd(const char* __restrict__ Q, const char* __restrict__ K, const char* __restrict__ V, char* __restrict__ O) {
  __shared__ __attribute__((aligned(1024))) char lds[131072];
  const unsigned wid = __builtin_amdgcn_readfirstlane(threadIdx.x >> 6);
  const unsigned head = blockIdx.x;
  asm volatile("" :: "s"((unsigned)(size_t)lds) : "memory");
  asm volatile(
    "s_load_dwordx8 s[88:95], %1, 0x0\n"
    "s_mov_b32 s64, %5\n"
    "s_mov_b32 s65, %6\n"
    "v_mbcnt_lo_u32_b32 v214, -1, 0\n"
    "v_mbcnt_hi_u32_b32 v214, -1, v214\n"
    "s_lshl_b32 s58, s64, 17\n"
    "s_sub_u32 s58, s58, 896\n"
    "s_ashr_i32 s67, s58, 31\n"
    "v_bfe_u32 v215, v214, 2, 3\n"
    "v_lshrrev_b32 v216, 5, v214\n"
    "v_lshlrev_b32 v217, 8, v215\n"
    "v_lshl_add_u32 v217, v216, 6, v217\n"
    "v_bfe_i32 v215, v214, 4, 1\n"
    "v_xor_b32 v216, v214, v215\n"
    "v_and_b32 v216, 3, v216\n"
    "v_lshl_add_u32 v219, v216, 4, v217\n"
    "s_and_b32 s68, s65, 1\n"
    "s_lshl_b32 s68, s68, 5\n"
    "s_lshl_b32 s59, s65, 11\n"
    "v_xor_b32 v211, s68, v219\n"
    "v_add_u32 v211, s59, v211\n"
    "s_add_u32 s81, s59, 65536\n"
    "s_mov_b32 s56, 896\n"
    "s_mov_b32 m0, s81\n"
    "s_add_u32 s82, s59, 98304\n"
    "s_lshl_b32 s57, s65, 14\n"
    "v_xor_b32 v220, 32, v219\n"
    "s_mov_b32 s46, 131968\n"
    "s_mov_b32 s47, 0x20000\n"
    "s_waitcnt lgkmcnt(0)\n"
    "s_add_u32 s44, s90, s58\n"
    "s_addc_u32 s45, s91, s67\n"
    "buffer_load_dwordx4 v211, s[44:47], s56 offen nt lds\n"
    "buffer_load_dwordx4 v211, s[44:47], 0 offen offset:1024 nt lds\n"
    "v_add_u32 v213, 0x2000, v211\n"
    "s_xor_b32 m0, s81, 0x2000\n"
    "s_nop 0\n"
    "buffer_load_dwordx4 v213, s[44:47], s56 offen nt lds\n"
    "buffer_load_dwordx4 v213, s[44:47], 0 offen offset:1024 nt lds\n"
    "v_add_u32 v213, 0x2000, v213\n"
    "s_lshl_b32 s57, s65, 14\n"
    "s_lshl_b32 s69, s64, 16\n"
    "s_lshl_b32 s68, s65, 12\n"
    "s_add_u32 s69, s69, s68\n"
    "s_sub_u32 s59, s69, 896\n"
    "s_ashr_i32 s68, s59, 31\n"
    "s_add_u32 s40, s88, s59\n"
    "s_addc_u32 s41, s89, s68\n"
    "s_mov_b32 s42, 54144\n"
    "s_mov_b32 s43, 0x20000\n"
    "v_xor_b32 v220, 32, v219\n"
    "s_mov_b32 m0, s57\n"
    "s_mov_b32 s70, 896\n"
    "s_mov_b32 s71, 0\n"
    "buffer_load_dwordx4 v219, s[40:43], s70 offen nt lds\n"
    "buffer_load_dwordx4 v219, s[40:43], s71 offen offset:1024 nt lds\n"
    "buffer_load_dwordx4 v220, s[40:43], s70 offen offset:2048 nt lds\n"
    "buffer_load_dwordx4 v220, s[40:43], s71 offen offset:3072 nt lds\n"
    "s_add_u32 m0, s57, 0x1000\n"
    "s_mov_b32 s70, 17280\n"
    "s_mov_b32 s71, 16384\n"
    "buffer_load_dwordx4 v219, s[40:43], s70 offen nt lds\n"
    "buffer_load_dwordx4 v219, s[40:43], s71 offen offset:1024 nt lds\n"
    "buffer_load_dwordx4 v220, s[40:43], s70 offen offset:2048 nt lds\n"
    "buffer_load_dwordx4 v220, s[40:43], s71 offen offset:3072 nt lds\n"
    "s_add_u32 s48, s92, s58\n"
    "s_addc_u32 s49, s93, s67\n"
    "s_mov_b32 s50, 131968\n"
    "s_mov_b32 s51, 0x20000\n"
    "s_add_u32 s52, s94, s69\n"
    "s_addc_u32 s53, s95, 0\n"
    "s_mov_b32 s54, 53248\n"
    "s_mov_b32 s55, 0x20000\n"
    "v_bfe_u32 v215, v214, 2, 1\n"
    "v_bfe_u32 v216, v214, 3, 1\n"
    "v_lshl_add_u32 v217, v215, 1, v216\n"
    "v_sub_u32 v217, 0, v217\n"
    "v_lshrrev_b32 v218, 4, v214\n"
    "v_xor_b32 v217, v217, v218\n"
    "v_and_b32 v217, 3, v217\n"
    "v_lshlrev_b32 v217, 4, v217\n"
    "v_lshl_add_u32 v217, v215, 11, v217\n"
    "v_lshl_add_u32 v217, v216, 8, v217\n"
    "v_and_b32 v215, 3, v214\n"
    "v_lshl_add_u32 v217, v215, 6, v217\n"
    "v_add_u32 v208, 0x10000, v217\n"
    "v_bfe_u32 v215, v214, 2, 2\n"
    "v_sub_u32 v215, 0, v215\n"
    "v_xor_b32 v215, v215, v218\n"
    "v_and_b32 v215, 3, v215\n"
    "v_lshlrev_b32 v215, 4, v215\n"
    "v_bfe_u32 v216, v214, 3, 1\n"
    "v_lshl_add_u32 v215, v216, 11, v215\n"
    "v_and_b32 v216, 7, v214\n"
    "v_lshl_add_u32 v215, v216, 6, v215\n"
    "v_add_u32 v221, s57, v215\n"
    "v_bfe_u32 v215, v214, 4, 1\n"
    "v_lshrrev_b32 v216, 5, v214\n"
    "v_lshl_add_u32 v217, v215, 1, v216\n"
    "v_sub_u32 v217, 0, v217\n"
    "v_bfe_u32 v218, v214, 1, 1\n"
    "v_xor_b32 v217, v217, v218\n"
    "v_and_b32 v217, 3, v217\n"
    "v_lshlrev_b32 v217, 4, v217\n"
    "v_lshl_add_u32 v217, v215, 11, v217\n"
    "v_lshl_add_u32 v217, v216, 8, v217\n"
    "v_bfe_u32 v215, v214, 2, 2\n"
    "v_lshl_add_u32 v217, v215, 6, v217\n"
    "v_and_b32 v215, 1, v214\n"
    "v_lshl_add_u32 v217, v215, 3, v217\n"
    "v_add_u32 v209, 0x18000, v217\n"
    "v_xor_b32 v210, 32, v209\n"
    "s_mov_b32 s72, 0x3e0293ee\n"
    "s_mov_b32 s73, 0x3e0293ee\n"
    "v_mov_b32 v226, 0\n"
    "v_mov_b32 v227, 0\n"
    "v_mov_b32 v228, 0\n"
    "v_mov_b32 v229, 0\n"
    "v_mov_b32 v230, 0x3f803f80\n"
    "v_mov_b32 v192, 0\n"
    "v_mov_b32 v193, 0\n"
    "v_mov_b32 v194, 0\n"
    "v_mov_b32 v195, 0\n"
    "v_mov_b32 v196, 0\n"
    "v_mov_b32 v197, 0\n"
    "v_mov_b32 v198, 0\n"
    "v_mov_b32 v199, 0\n"
    "v_mov_b32 v200, 0\n"
    "v_mov_b32 v201, 0\n"
    "v_mov_b32 v202, 0\n"
    "v_mov_b32 v203, 0\n"
    "v_mov_b32 v204, 0\n"
    "v_mov_b32 v205, 0\n"
    "v_mov_b32 v206, 0\n"
    "v_mov_b32 v207, 0\n"
    "v_accvgpr_write_b32 a160, v230\n"
    "v_accvgpr_write_b32 a161, v230\n"
    "v_accvgpr_write_b32 a162, v230\n"
    "v_accvgpr_write_b32 a163, v230\n"
    "v_mfma_f32_32x32x16_bf16 v[0:15], v[226:229], v[226:229], 0\n"
    "v_mfma_f32_32x32x16_bf16 v[16:31], v[226:229], v[226:229], 0\n"
    "v_mfma_f32_32x32x16_bf16 v[32:47], v[226:229], v[226:229], 0\n"
    "v_mfma_f32_32x32x16_bf16 v[48:63], v[226:229], v[226:229], 0\n"
    "v_mfma_f32_32x32x16_bf16 v[64:79], v[226:229], v[226:229], 0\n"
    "v_mfma_f32_32x32x16_bf16 v[80:95], v[226:229], v[226:229], 0\n"
    "v_mfma_f32_32x32x16_bf16 v[96:111], v[226:229], v[226:229], 0\n"
    "v_mfma_f32_32x32x16_bf16 v[112:127], v[226:229], v[226:229], 0\n"
    "s_waitcnt vmcnt(8)\n"
    "s_add_u32 m0, s57, 0x2000\n"
    "s_mov_b32 s70, 33664\n"
    "s_mov_b32 s71, 32768\n"
    "buffer_load_dwordx4 v219, s[40:43], s70 offen nt lds\n"
    "buffer_load_dwordx4 v219, s[40:43], s71 offen offset:1024 nt lds\n"
    "buffer_load_dwordx4 v220, s[40:43], s70 offen offset:2048 nt lds\n"
    "buffer_load_dwordx4 v220, s[40:43], s71 offen offset:3072 nt lds\n"
    "s_barrier\n"
    "ds_read_b128 a[128:131], v208 offset:0\n"
    "ds_read_b128 a[132:135], v208 offset:512\n"
    "ds_read_b128 a[136:139], v208 offset:1024\n"
    "ds_read_b128 a[140:143], v208 offset:1536\n"
    "ds_read_b128 a[144:147], v208 offset:4096\n"
    "ds_read_b128 a[148:151], v208 offset:4608\n"
    "ds_read_b128 a[152:155], v208 offset:5120\n"
    "ds_read_b128 a[156:159], v208 offset:5632\n"
    "ds_read_b128 a[64:67], v208 offset:8192\n"
    "ds_read_b128 a[68:71], v208 offset:8704\n"
    "ds_read_b128 a[72:75], v208 offset:9216\n"
    "ds_read_b128 a[76:79], v208 offset:9728\n"
    "ds_read_b128 a[80:83], v208 offset:12288\n"
    "ds_read_b128 a[84:87], v208 offset:12800\n"
    "ds_read_b128 a[88:91], v208 offset:13312\n"
    "ds_read_b128 a[92:95], v208 offset:13824\n"
    "v_xor_b32 v208, 0x6000, v208\n"
    "s_waitcnt vmcnt(8)\n"
    "ds_read_b128 v[224:227], v221 offset:0\n"
    "ds_read_b128 v[228:231], v221 offset:512\n"
    "ds_read_b128 v[232:235], v221 offset:1024\n"
    "ds_read_b128 v[236:239], v221 offset:1536\n"
    "s_add_u32 m0, s57, 0x3000\n"
    "s_mov_b32 s70, 50048\n"
    "s_mov_b32 s71, 49152\n"
    "buffer_load_dwordx4 v219, s[40:43], s70 offen nt lds\n"
    "buffer_load_dwordx4 v219, s[40:43], s71 offen offset:1024 nt lds\n"
    "buffer_load_dwordx4 v220, s[40:43], s70 offen offset:2048 nt lds\n"
    "buffer_load_dwordx4 v220, s[40:43], s71 offen offset:3072 nt lds\n"
    "s_waitcnt lgkmcnt(0)\n"
    "v_lshlrev_b32 v240, 16, v224\n"
    "v_lshlrev_b32 v242, 16, v225\n"
    "v_lshlrev_b32 v244, 16, v226\n"
    "v_lshlrev_b32 v246, 16, v227\n"
    "v_and_b32 v241, 0xffff0000, v224\n"
    "v_and_b32 v243, 0xffff0000, v225\n"
    "v_and_b32 v245, 0xffff0000, v226\n"
    "v_and_b32 v247, 0xffff0000, v227\n"
    "v_mul_f32 v240, s72, v240\n"
    "v_mul_f32 v242, s72, v242\n"
    "v_mul_f32 v244, s72, v244\n"
    "v_mul_f32 v246, s72, v246\n"
    "v_mul_f32 v241, s72, v241\n"
    "v_mul_f32 v243, s72, v243\n"
    "v_mul_f32 v245, s72, v245\n"
    "v_mul_f32 v247, s72, v247\n"
    "v_cvt_pk_bf16_f32 v224, v240, v241\n"
    "v_cvt_pk_bf16_f32 v225, v242, v243\n"
    "v_cvt_pk_bf16_f32 v226, v244, v245\n"
    "v_cvt_pk_bf16_f32 v227, v246, v247\n"
    "v_accvgpr_write_b32 a0, v224\n"
    "v_accvgpr_write_b32 a1, v225\n"
    "v_accvgpr_write_b32 a2, v226\n"
    "v_accvgpr_write_b32 a3, v227\n"
    "v_lshlrev_b32 v240, 16, v228\n"
    "v_lshlrev_b32 v242, 16, v229\n"
    "v_lshlrev_b32 v244, 16, v230\n"
    "v_lshlrev_b32 v246, 16, v231\n"
    "v_and_b32 v241, 0xffff0000, v228\n"
    "v_and_b32 v243, 0xffff0000, v229\n"
    "v_and_b32 v245, 0xffff0000, v230\n"
    "v_and_b32 v247, 0xffff0000, v231\n"
    "v_mul_f32 v240, s72, v240\n"
    "v_mul_f32 v242, s72, v242\n"
    "v_mul_f32 v244, s72, v244\n"
    "v_mul_f32 v246, s72, v246\n"
    "v_mul_f32 v241, s72, v241\n"
    "v_mul_f32 v243, s72, v243\n"
    "v_mul_f32 v245, s72, v245\n"
    "v_mul_f32 v247, s72, v247\n"
    "v_cvt_pk_bf16_f32 v228, v240, v241\n"
    "v_cvt_pk_bf16_f32 v229, v242, v243\n"
    "v_cvt_pk_bf16_f32 v230, v244, v245\n"
    "v_cvt_pk_bf16_f32 v231, v246, v247\n"
    "v_accvgpr_write_b32 a4, v228\n"
    "v_accvgpr_write_b32 a5, v229\n"
    "v_accvgpr_write_b32 a6, v230\n"
    "v_accvgpr_write_b32 a7, v231\n"
    "v_lshlrev_b32 v240, 16, v232\n"
    "v_lshlrev_b32 v242, 16, v233\n"
    "v_lshlrev_b32 v244, 16, v234\n"
    "v_lshlrev_b32 v246, 16, v235\n"
    "v_and_b32 v241, 0xffff0000, v232\n"
    "v_and_b32 v243, 0xffff0000, v233\n"
    "v_and_b32 v245, 0xffff0000, v234\n"
    "v_and_b32 v247, 0xffff0000, v235\n"
    "v_mul_f32 v240, s72, v240\n"
    "v_mul_f32 v242, s72, v242\n"
    "v_mul_f32 v244, s72, v244\n"
    "v_mul_f32 v246, s72, v246\n"
    "v_mul_f32 v241, s72, v241\n"
    "v_mul_f32 v243, s72, v243\n"
    "v_mul_f32 v245, s72, v245\n"
    "v_mul_f32 v247, s72, v247\n"
    "v_cvt_pk_bf16_f32 v232, v240, v241\n"
    "v_cvt_pk_bf16_f32 v233, v242, v243\n"
    "v_cvt_pk_bf16_f32 v234, v244, v245\n"
    "v_cvt_pk_bf16_f32 v235, v246, v247\n"
    "v_accvgpr_write_b32 a8, v232\n"
    "v_accvgpr_write_b32 a9, v233\n"
    "v_accvgpr_write_b32 a10, v234\n"
    "v_accvgpr_write_b32 a11, v235\n"
    "v_lshlrev_b32 v240, 16, v236\n"
    "v_lshlrev_b32 v242, 16, v237\n"
    "v_lshlrev_b32 v244, 16, v238\n"
    "v_lshlrev_b32 v246, 16, v239\n"
    "v_and_b32 v241, 0xffff0000, v236\n"
    "v_and_b32 v243, 0xffff0000, v237\n"
    "v_and_b32 v245, 0xffff0000, v238\n"
    "v_and_b32 v247, 0xffff0000, v239\n"
    "v_mul_f32 v240, s72, v240\n"
    "v_mul_f32 v242, s72, v242\n"
    "v_mul_f32 v244, s72, v244\n"
    "v_mul_f32 v246, s72, v246\n"
    "v_mul_f32 v241, s72, v241\n"
    "v_mul_f32 v243, s72, v243\n"
    "v_mul_f32 v245, s72, v245\n"
    "v_mul_f32 v247, s72, v247\n"
    "v_cvt_pk_bf16_f32 v236, v240, v241\n"
    "v_cvt_pk_bf16_f32 v237, v242, v243\n"
    "v_cvt_pk_bf16_f32 v238, v244, v245\n"
    "v_cvt_pk_bf16_f32 v239, v246, v247\n"
    "v_accvgpr_write_b32 a12, v236\n"
    "v_accvgpr_write_b32 a13, v237\n"
    "v_accvgpr_write_b32 a14, v238\n"
    "v_accvgpr_write_b32 a15, v239\n"
    "v_mfma_f32_16x16x32_bf16 v[128:131], a[128:131], a[0:3], 0\n"
    "v_mfma_f32_16x16x32_bf16 v[128:131], a[132:135], a[4:7], v[128:131]\n"
    "v_mfma_f32_16x16x32_bf16 v[128:131], a[136:139], a[8:11], v[128:131]\n"
    "v_mfma_f32_16x16x32_bf16 v[128:131], a[140:143], a[12:15], v[128:131]\n"
    "v_mfma_f32_16x16x32_bf16 v[132:135], a[144:147], a[0:3], 0\n"
    "v_mfma_f32_16x16x32_bf16 v[132:135], a[148:151], a[4:7], v[132:135]\n"
    "v_mfma_f32_16x16x32_bf16 v[132:135], a[152:155], a[8:11], v[132:135]\n"
    "v_mfma_f32_16x16x32_bf16 v[132:135], a[156:159], a[12:15], v[132:135]\n"
    "s_waitcnt vmcnt(8)\n"
    "ds_read_b128 v[224:227], v221 offset:4096\n"
    "ds_read_b128 v[228:231], v221 offset:4608\n"
    "ds_read_b128 v[232:235], v221 offset:5120\n"
    "ds_read_b128 v[236:239], v221 offset:5632\n"
    "s_mov_b32 m0, s82\n"
    "s_nop 0\n"
    "buffer_load_dwordx4 v211, s[48:51], s56 offen nt lds\n"
    "buffer_load_dwordx4 v211, s[48:51], 0 offen offset:1024 nt lds\n"
    "v_add_u32 v211, 0x2000, v211\n"
    "s_xor_b32 m0, s81, 0x6000\n"
    "s_nop 0\n"
    "buffer_load_dwordx4 v213, s[44:47], s56 offen nt lds\n"
    "buffer_load_dwordx4 v213, s[44:47], 0 offen offset:1024 nt lds\n"
    "v_add_u32 v213, 0x2000, v213\n"
    "v_max3_f32 v248, v128, v129, v130\n"
    "v_max3_f32 v248, v248, v131, v132\n"
    "v_max3_f32 v248, v248, v133, v134\n"
    "v_max_f32 v248, v248, v135\n"
    "v_mov_b32 v252, v248\n"
    "s_nop 1\n"
    "v_permlane32_swap_b32 v248, v252\n"
    "v_max_f32 v248, v248, v252\n"
    "v_mov_b32 v252, v248\n"
    "s_nop 1\n"
    "v_permlane16_swap_b32 v248, v252\n"
    "v_max_f32 v248, v248, v252\n"
    "v_sub_f32 v160, 0, v248\n"
    "v_sub_f32 v161, 0, v248\n"
    "v_sub_f32 v162, 0, v248\n"
    "v_sub_f32 v163, 0, v248\n"
    "v_sub_f32 v128, v128, v248\n"
    "v_sub_f32 v129, v129, v248\n"
    "v_sub_f32 v130, v130, v248\n"
    "v_sub_f32 v131, v131, v248\n"
    "v_sub_f32 v132, v132, v248\n"
    "v_sub_f32 v133, v133, v248\n"
    "v_sub_f32 v134, v134, v248\n"
    "v_sub_f32 v135, v135, v248\n"
    "v_exp_f32 v128, v128\n"
    "v_exp_f32 v129, v129\n"
    "v_exp_f32 v130, v130\n"
    "v_exp_f32 v131, v131\n"
    "v_exp_f32 v132, v132\n"
    "v_exp_f32 v133, v133\n"
    "v_exp_f32 v134, v134\n"
    "v_exp_f32 v135, v135\n"
    "v_cvt_pk_bf16_f32 v176, v128, v129\n"
    "v_cvt_pk_bf16_f32 v177, v130, v131\n"
    "v_cvt_pk_bf16_f32 v178, v132, v133\n"
    "v_cvt_pk_bf16_f32 v179, v134, v135\n"
    "v_mfma_f32_16x16x32_bf16 v[128:131], a[64:67], a[0:3], v[160:163]\n"
    "v_mfma_f32_16x16x32_bf16 v[128:131], a[68:71], a[4:7], v[128:131]\n"
    "v_mfma_f32_16x16x32_bf16 v[128:131], a[72:75], a[8:11], v[128:131]\n"
    "v_mfma_f32_16x16x32_bf16 v[128:131], a[76:79], a[12:15], v[128:131]\n"
    "v_mfma_f32_16x16x32_bf16 v[132:135], a[80:83], a[0:3], v[160:163]\n"
    "v_mfma_f32_16x16x32_bf16 v[132:135], a[84:87], a[4:7], v[132:135]\n"
    "v_mfma_f32_16x16x32_bf16 v[132:135], a[88:91], a[8:11], v[132:135]\n"
    "v_mfma_f32_16x16x32_bf16 v[132:135], a[92:95], a[12:15], v[132:135]\n"
    "s_waitcnt lgkmcnt(0)\n"
    "v_lshlrev_b32 v240, 16, v224\n"
    "v_lshlrev_b32 v242, 16, v225\n"
    "v_lshlrev_b32 v244, 16, v226\n"
    "v_lshlrev_b32 v246, 16, v227\n"
    "v_and_b32 v241, 0xffff0000, v224\n"
    "v_and_b32 v243, 0xffff0000, v225\n"
    "v_and_b32 v245, 0xffff0000, v226\n"
    "v_and_b32 v247, 0xffff0000, v227\n"
    "v_mul_f32 v240, s72, v240\n"
    "v_mul_f32 v242, s72, v242\n"
    "v_mul_f32 v244, s72, v244\n"
    "v_mul_f32 v246, s72, v246\n"
    "v_mul_f32 v241, s72, v241\n"
    "v_mul_f32 v243, s72, v243\n"
    "v_mul_f32 v245, s72, v245\n"
    "v_mul_f32 v247, s72, v247\n"
    "v_cvt_pk_bf16_f32 v224, v240, v241\n"
    "v_cvt_pk_bf16_f32 v225, v242, v243\n"
    "v_cvt_pk_bf16_f32 v226, v244, v245\n"
    "v_cvt_pk_bf16_f32 v227, v246, v247\n"
    "v_accvgpr_write_b32 a16, v224\n"
    "v_accvgpr_write_b32 a17, v225\n"
    "v_accvgpr_write_b32 a18, v226\n"
    "v_accvgpr_write_b32 a19, v227\n"
    "v_lshlrev_b32 v240, 16, v228\n"
    "v_lshlrev_b32 v242, 16, v229\n"
    "v_lshlrev_b32 v244, 16, v230\n"
    "v_lshlrev_b32 v246, 16, v231\n"
    "v_and_b32 v241, 0xffff0000, v228\n"
    "v_and_b32 v243, 0xffff0000, v229\n"
    "v_and_b32 v245, 0xffff0000, v230\n"
    "v_and_b32 v247, 0xffff0000, v231\n"
    "v_mul_f32 v240, s72, v240\n"
    "v_mul_f32 v242, s72, v242\n"
    "v_mul_f32 v244, s72, v244\n"
    "v_mul_f32 v246, s72, v246\n"
    "v_mul_f32 v241, s72, v241\n"
    "v_mul_f32 v243, s72, v243\n"
    "v_mul_f32 v245, s72, v245\n"
    "v_mul_f32 v247, s72, v247\n"
    "v_cvt_pk_bf16_f32 v228, v240, v241\n"
    "v_cvt_pk_bf16_f32 v229, v242, v243\n"
    "v_cvt_pk_bf16_f32 v230, v244, v245\n"
    "v_cvt_pk_bf16_f32 v231, v246, v247\n"
    "v_accvgpr_write_b32 a20, v228\n"
    "v_accvgpr_write_b32 a21, v229\n"
    "v_accvgpr_write_b32 a22, v230\n"
    "v_accvgpr_write_b32 a23, v231\n"
    "v_lshlrev_b32 v240, 16, v232\n"
    "v_lshlrev_b32 v242, 16, v233\n"
    "v_lshlrev_b32 v244, 16, v234\n"
    "v_lshlrev_b32 v246, 16, v235\n"
    "v_and_b32 v241, 0xffff0000, v232\n"
    "v_and_b32 v243, 0xffff0000, v233\n"
    "v_and_b32 v245, 0xffff0000, v234\n"
    "v_and_b32 v247, 0xffff0000, v235\n"
    "v_mul_f32 v240, s72, v240\n"
    "v_mul_f32 v242, s72, v242\n"
    "v_mul_f32 v244, s72, v244\n"
    "v_mul_f32 v246, s72, v246\n"
    "v_mul_f32 v241, s72, v241\n"
    "v_mul_f32 v243, s72, v243\n"
    "v_mul_f32 v245, s72, v245\n"
    "v_mul_f32 v247, s72, v247\n"
    "v_cvt_pk_bf16_f32 v232, v240, v241\n"
    "v_cvt_pk_bf16_f32 v233, v242, v243\n"
    "v_cvt_pk_bf16_f32 v234, v244, v245\n"
    "v_cvt_pk_bf16_f32 v235, v246, v247\n"
    "v_accvgpr_write_b32 a24, v232\n"
    "v_accvgpr_write_b32 a25, v233\n"
    "v_accvgpr_write_b32 a26, v234\n"
    "v_accvgpr_write_b32 a27, v235\n"
    "v_lshlrev_b32 v240, 16, v236\n"
    "v_lshlrev_b32 v242, 16, v237\n"
    "v_lshlrev_b32 v244, 16, v238\n"
    "v_lshlrev_b32 v246, 16, v239\n"
    "v_and_b32 v241, 0xffff0000, v236\n"
    "v_and_b32 v243, 0xffff0000, v237\n"
    "v_and_b32 v245, 0xffff0000, v238\n"
    "v_and_b32 v247, 0xffff0000, v239\n"
    "v_mul_f32 v240, s72, v240\n"
    "v_mul_f32 v242, s72, v242\n"
    "v_mul_f32 v244, s72, v244\n"
    "v_mul_f32 v246, s72, v246\n"
    "v_mul_f32 v241, s72, v241\n"
    "v_mul_f32 v243, s72, v243\n"
    "v_mul_f32 v245, s72, v245\n"
    "v_mul_f32 v247, s72, v247\n"
    "v_cvt_pk_bf16_f32 v236, v240, v241\n"
    "v_cvt_pk_bf16_f32 v237, v242, v243\n"
    "v_cvt_pk_bf16_f32 v238, v244, v245\n"
    "v_cvt_pk_bf16_f32 v239, v246, v247\n"
    "v_accvgpr_write_b32 a28, v236\n"
    "v_accvgpr_write_b32 a29, v237\n"
    "v_accvgpr_write_b32 a30, v238\n"
    "v_accvgpr_write_b32 a31, v239\n"
    "v_mfma_f32_16x16x32_bf16 v[136:139], a[128:131], a[16:19], 0\n"
    "v_mfma_f32_16x16x32_bf16 v[136:139], a[132:135], a[20:23], v[136:139]\n"
    "v_mfma_f32_16x16x32_bf16 v[136:139], a[136:139], a[24:27], v[136:139]\n"
    "v_mfma_f32_16x16x32_bf16 v[136:139], a[140:143], a[28:31], v[136:139]\n"
    "v_mfma_f32_16x16x32_bf16 v[140:143], a[144:147], a[16:19], 0\n"
    "v_mfma_f32_16x16x32_bf16 v[140:143], a[148:151], a[20:23], v[140:143]\n"
    "v_mfma_f32_16x16x32_bf16 v[140:143], a[152:155], a[24:27], v[140:143]\n"
    "v_mfma_f32_16x16x32_bf16 v[140:143], a[156:159], a[28:31], v[140:143]\n"
    "s_waitcnt vmcnt(8)\n"
    "ds_read_b128 v[224:227], v221 offset:8192\n"
    "ds_read_b128 v[228:231], v221 offset:8704\n"
    "ds_read_b128 v[232:235], v221 offset:9216\n"
    "ds_read_b128 v[236:239], v221 offset:9728\n"
    "s_xor_b32 m0, s82, 0x2000\n"
    "s_nop 0\n"
    "buffer_load_dwordx4 v211, s[48:51], s56 offen nt lds\n"
    "buffer_load_dwordx4 v211, s[48:51], 0 offen offset:1024 nt lds\n"
    "v_add_u32 v211, 0x2000, v211\n"
    "s_xor_b32 m0, s81, 0x4000\n"
    "s_nop 0\n"
    "buffer_load_dwordx4 v213, s[44:47], s56 offen nt lds\n"
    "buffer_load_dwordx4 v213, s[44:47], 0 offen offset:1024 nt lds\n"
    "v_add_u32 v213, 0x2000, v213\n"
    "v_exp_f32 v128, v128\n"
    "v_exp_f32 v129, v129\n"
    "v_exp_f32 v130, v130\n"
    "v_exp_f32 v131, v131\n"
    "v_exp_f32 v132, v132\n"
    "v_exp_f32 v133, v133\n"
    "v_exp_f32 v134, v134\n"
    "v_exp_f32 v135, v135\n"
    "v_max3_f32 v249, v136, v137, v138\n"
    "v_max3_f32 v249, v249, v139, v140\n"
    "v_max3_f32 v249, v249, v141, v142\n"
    "v_max_f32 v249, v249, v143\n"
    "v_mov_b32 v252, v249\n"
    "s_nop 1\n"
    "v_permlane32_swap_b32 v249, v252\n"
    "v_max_f32 v249, v249, v252\n"
    "v_mov_b32 v252, v249\n"
    "s_nop 1\n"
    "v_permlane16_swap_b32 v249, v252\n"
    "v_max_f32 v249, v249, v252\n"
    "v_sub_f32 v164, 0, v249\n"
    "v_sub_f32 v165, 0, v249\n"
    "v_sub_f32 v166, 0, v249\n"
    "v_sub_f32 v167, 0, v249\n"
    "v_sub_f32 v136, v136, v249\n"
    "v_sub_f32 v137, v137, v249\n"
    "v_sub_f32 v138, v138, v249\n"
    "v_sub_f32 v139, v139, v249\n"
    "v_sub_f32 v140, v140, v249\n"
    "v_sub_f32 v141, v141, v249\n"
    "v_sub_f32 v142, v142, v249\n"
    "v_sub_f32 v143, v143, v249\n"
    "v_exp_f32 v136, v136\n"
    "v_exp_f32 v137, v137\n"
    "v_exp_f32 v138, v138\n"
    "v_exp_f32 v139, v139\n"
    "v_exp_f32 v140, v140\n"
    "v_exp_f32 v141, v141\n"
    "v_exp_f32 v142, v142\n"
    "v_exp_f32 v143, v143\n"
    "v_cvt_pk_bf16_f32 v180, v136, v137\n"
    "v_cvt_pk_bf16_f32 v181, v138, v139\n"
    "v_cvt_pk_bf16_f32 v182, v140, v141\n"
    "v_cvt_pk_bf16_f32 v183, v142, v143\n"
    "v_mfma_f32_16x16x32_bf16 v[136:139], a[64:67], a[16:19], v[164:167]\n"
    "v_mfma_f32_16x16x32_bf16 v[136:139], a[68:71], a[20:23], v[136:139]\n"
    "v_mfma_f32_16x16x32_bf16 v[136:139], a[72:75], a[24:27], v[136:139]\n"
    "v_mfma_f32_16x16x32_bf16 v[136:139], a[76:79], a[28:31], v[136:139]\n"
    "v_mfma_f32_16x16x32_bf16 v[140:143], a[80:83], a[16:19], v[164:167]\n"
    "v_mfma_f32_16x16x32_bf16 v[140:143], a[84:87], a[20:23], v[140:143]\n"
    "v_mfma_f32_16x16x32_bf16 v[140:143], a[88:91], a[24:27], v[140:143]\n"
    "v_mfma_f32_16x16x32_bf16 v[140:143], a[92:95], a[28:31], v[140:143]\n"
    "s_waitcnt lgkmcnt(0)\n"
    "v_lshlrev_b32 v240, 16, v224\n"
    "v_lshlrev_b32 v242, 16, v225\n"
    "v_lshlrev_b32 v244, 16, v226\n"
    "v_lshlrev_b32 v246, 16, v227\n"
    "v_and_b32 v241, 0xffff0000, v224\n"
    "v_and_b32 v243, 0xffff0000, v225\n"
    "v_and_b32 v245, 0xffff0000, v226\n"
    "v_and_b32 v247, 0xffff0000, v227\n"
    "v_mul_f32 v240, s72, v240\n"
    "v_mul_f32 v242, s72, v242\n"
    "v_mul_f32 v244, s72, v244\n"
    "v_mul_f32 v246, s72, v246\n"
    "v_mul_f32 v241, s72, v241\n"
    "v_mul_f32 v243, s72, v243\n"
    "v_mul_f32 v245, s72, v245\n"
    "v_mul_f32 v247, s72, v247\n"
    "v_cvt_pk_bf16_f32 v224, v240, v241\n"
    "v_cvt_pk_bf16_f32 v225, v242, v243\n"
    "v_cvt_pk_bf16_f32 v226, v244, v245\n"
    "v_cvt_pk_bf16_f32 v227, v246, v247\n"
    "v_accvgpr_write_b32 a32, v224\n"
    "v_accvgpr_write_b32 a33, v225\n"
    "v_accvgpr_write_b32 a34, v226\n"
    "v_accvgpr_write_b32 a35, v227\n"
    "v_lshlrev_b32 v240, 16, v228\n"
    "v_lshlrev_b32 v242, 16, v229\n"
    "v_lshlrev_b32 v244, 16, v230\n"
    "v_lshlrev_b32 v246, 16, v231\n"
    "v_and_b32 v241, 0xffff0000, v228\n"
    "v_and_b32 v243, 0xffff0000, v229\n"
    "v_and_b32 v245, 0xffff0000, v230\n"
    "v_and_b32 v247, 0xffff0000, v231\n"
    "v_mul_f32 v240, s72, v240\n"
    "v_mul_f32 v242, s72, v242\n"
    "v_mul_f32 v244, s72, v244\n"
    "v_mul_f32 v246, s72, v246\n"
    "v_mul_f32 v241, s72, v241\n"
    "v_mul_f32 v243, s72, v243\n"
    "v_mul_f32 v245, s72, v245\n"
    "v_mul_f32 v247, s72, v247\n"
    "v_cvt_pk_bf16_f32 v228, v240, v241\n"
    "v_cvt_pk_bf16_f32 v229, v242, v243\n"
    "v_cvt_pk_bf16_f32 v230, v244, v245\n"
    "v_cvt_pk_bf16_f32 v231, v246, v247\n"
    "v_accvgpr_write_b32 a36, v228\n"
    "v_accvgpr_write_b32 a37, v229\n"
    "v_accvgpr_write_b32 a38, v230\n"
    "v_accvgpr_write_b32 a39, v231\n"
    "v_lshlrev_b32 v240, 16, v232\n"
    "v_lshlrev_b32 v242, 16, v233\n"
    "v_lshlrev_b32 v244, 16, v234\n"
    "v_lshlrev_b32 v246, 16, v235\n"
    "v_and_b32 v241, 0xffff0000, v232\n"
    "v_and_b32 v243, 0xffff0000, v233\n"
    "v_and_b32 v245, 0xffff0000, v234\n"
    "v_and_b32 v247, 0xffff0000, v235\n"
    "v_mul_f32 v240, s72, v240\n"
    "v_mul_f32 v242, s72, v242\n"
    "v_mul_f32 v244, s72, v244\n"
    "v_mul_f32 v246, s72, v246\n"
    "v_mul_f32 v241, s72, v241\n"
    "v_mul_f32 v243, s72, v243\n"
    "v_mul_f32 v245, s72, v245\n"
    "v_mul_f32 v247, s72, v247\n"
    "v_cvt_pk_bf16_f32 v232, v240, v241\n"
    "v_cvt_pk_bf16_f32 v233, v242, v243\n"
    "v_cvt_pk_bf16_f32 v234, v244, v245\n"
    "v_cvt_pk_bf16_f32 v235, v246, v247\n"
    "v_accvgpr_write_b32 a40, v232\n"
    "v_accvgpr_write_b32 a41, v233\n"
    "v_accvgpr_write_b32 a42, v234\n"
    "v_accvgpr_write_b32 a43, v235\n"
    "v_lshlrev_b32 v240, 16, v236\n"
    "v_lshlrev_b32 v242, 16, v237\n"
    "v_lshlrev_b32 v244, 16, v238\n"
    "v_lshlrev_b32 v246, 16, v239\n"
    "v_and_b32 v241, 0xffff0000, v236\n"
    "v_and_b32 v243, 0xffff0000, v237\n"
    "v_and_b32 v245, 0xffff0000, v238\n"
    "v_and_b32 v247, 0xffff0000, v239\n"
    "v_mul_f32 v240, s72, v240\n"
    "v_mul_f32 v242, s72, v242\n"
    "v_mul_f32 v244, s72, v244\n"
    "v_mul_f32 v246, s72, v246\n"
    "v_mul_f32 v241, s72, v241\n"
    "v_mul_f32 v243, s72, v243\n"
    "v_mul_f32 v245, s72, v245\n"
    "v_mul_f32 v247, s72, v247\n"
    "v_cvt_pk_bf16_f32 v236, v240, v241\n"
    "v_cvt_pk_bf16_f32 v237, v242, v243\n"
    "v_cvt_pk_bf16_f32 v238, v244, v245\n"
    "v_cvt_pk_bf16_f32 v239, v246, v247\n"
    "v_accvgpr_write_b32 a44, v236\n"
    "v_accvgpr_write_b32 a45, v237\n"
    "v_accvgpr_write_b32 a46, v238\n"
    "v_accvgpr_write_b32 a47, v239\n"
    "v_mfma_f32_16x16x32_bf16 v[144:147], a[128:131], a[32:35], 0\n"
    "v_mfma_f32_16x16x32_bf16 v[144:147], a[132:135], a[36:39], v[144:147]\n"
    "v_mfma_f32_16x16x32_bf16 v[144:147], a[136:139], a[40:43], v[144:147]\n"
    "v_mfma_f32_16x16x32_bf16 v[144:147], a[140:143], a[44:47], v[144:147]\n"
    "v_mfma_f32_16x16x32_bf16 v[148:151], a[144:147], a[32:35], 0\n"
    "v_mfma_f32_16x16x32_bf16 v[148:151], a[148:151], a[36:39], v[148:151]\n"
    "v_mfma_f32_16x16x32_bf16 v[148:151], a[152:155], a[40:43], v[148:151]\n"
    "v_mfma_f32_16x16x32_bf16 v[148:151], a[156:159], a[44:47], v[148:151]\n"
    "s_waitcnt vmcnt(8)\n"
    "ds_read_b128 v[224:227], v221 offset:12288\n"
    "ds_read_b128 v[228:231], v221 offset:12800\n"
    "ds_read_b128 v[232:235], v221 offset:13312\n"
    "ds_read_b128 v[236:239], v221 offset:13824\n"
    "s_xor_b32 m0, s82, 0x6000\n"
    "s_nop 0\n"
    "buffer_load_dwordx4 v211, s[48:51], s56 offen nt lds\n"
    "buffer_load_dwordx4 v211, s[48:51], 0 offen offset:1024 nt lds\n"
    "v_add_u32 v211, 0x2000, v211\n"
    "v_exp_f32 v136, v136\n"
    "v_exp_f32 v137, v137\n"
    "v_max3_f32 v250, v144, v145, v146\n"
    "v_max3_f32 v250, v250, v147, v148\n"
    "v_max3_f32 v250, v250, v149, v150\n"
    "v_max_f32 v250, v250, v151\n"
    "v_mov_b32 v252, v250\n"
    "s_nop 1\n"
    "v_permlane32_swap_b32 v250, v252\n"
    "v_max_f32 v250, v250, v252\n"
    "v_mov_b32 v252, v250\n"
    "s_nop 1\n"
    "v_permlane16_swap_b32 v250, v252\n"
    "v_max_f32 v250, v250, v252\n"
    "v_sub_f32 v168, 0, v250\n"
    "v_sub_f32 v169, 0, v250\n"
    "v_sub_f32 v170, 0, v250\n"
    "v_sub_f32 v171, 0, v250\n"
    "v_sub_f32 v144, v144, v250\n"
    "v_sub_f32 v145, v145, v250\n"
    "v_sub_f32 v146, v146, v250\n"
    "v_sub_f32 v147, v147, v250\n"
    "v_sub_f32 v148, v148, v250\n"
    "v_sub_f32 v149, v149, v250\n"
    "v_sub_f32 v150, v150, v250\n"
    "v_sub_f32 v151, v151, v250\n"
    "v_exp_f32 v144, v144\n"
    "v_exp_f32 v145, v145\n"
    "v_exp_f32 v146, v146\n"
    "v_exp_f32 v147, v147\n"
    "v_exp_f32 v148, v148\n"
    "v_exp_f32 v149, v149\n"
    "v_exp_f32 v150, v150\n"
    "v_exp_f32 v151, v151\n"
    "v_cvt_pk_bf16_f32 v184, v144, v145\n"
    "v_cvt_pk_bf16_f32 v185, v146, v147\n"
    "v_cvt_pk_bf16_f32 v186, v148, v149\n"
    "v_cvt_pk_bf16_f32 v187, v150, v151\n"
    "v_mfma_f32_16x16x32_bf16 v[144:147], a[64:67], a[32:35], v[168:171]\n"
    "v_mfma_f32_16x16x32_bf16 v[144:147], a[68:71], a[36:39], v[144:147]\n"
    "v_mfma_f32_16x16x32_bf16 v[144:147], a[72:75], a[40:43], v[144:147]\n"
    "v_mfma_f32_16x16x32_bf16 v[144:147], a[76:79], a[44:47], v[144:147]\n"
    "v_mfma_f32_16x16x32_bf16 v[148:151], a[80:83], a[32:35], v[168:171]\n"
    "v_mfma_f32_16x16x32_bf16 v[148:151], a[84:87], a[36:39], v[148:151]\n"
    "v_mfma_f32_16x16x32_bf16 v[148:151], a[88:91], a[40:43], v[148:151]\n"
    "v_mfma_f32_16x16x32_bf16 v[148:151], a[92:95], a[44:47], v[148:151]\n"
    "s_waitcnt lgkmcnt(0)\n"
    "v_lshlrev_b32 v240, 16, v224\n"
    "v_lshlrev_b32 v242, 16, v225\n"
    "v_lshlrev_b32 v244, 16, v226\n"
    "v_lshlrev_b32 v246, 16, v227\n"
    "v_and_b32 v241, 0xffff0000, v224\n"
    "v_and_b32 v243, 0xffff0000, v225\n"
    "v_and_b32 v245, 0xffff0000, v226\n"
    "v_and_b32 v247, 0xffff0000, v227\n"
    "v_mul_f32 v240, s72, v240\n"
    "v_mul_f32 v242, s72, v242\n"
    "v_mul_f32 v244, s72, v244\n"
    "v_mul_f32 v246, s72, v246\n"
    "v_mul_f32 v241, s72, v241\n"
    "v_mul_f32 v243, s72, v243\n"
    "v_mul_f32 v245, s72, v245\n"
    "v_mul_f32 v247, s72, v247\n"
    "v_cvt_pk_bf16_f32 v224, v240, v241\n"
    "v_cvt_pk_bf16_f32 v225, v242, v243\n"
    "v_cvt_pk_bf16_f32 v226, v244, v245\n"
    "v_cvt_pk_bf16_f32 v227, v246, v247\n"
    "v_accvgpr_write_b32 a48, v224\n"
    "v_accvgpr_write_b32 a49, v225\n"
    "v_accvgpr_write_b32 a50, v226\n"
    "v_accvgpr_write_b32 a51, v227\n"
    "v_lshlrev_b32 v240, 16, v228\n"
    "v_lshlrev_b32 v242, 16, v229\n"
    "v_lshlrev_b32 v244, 16, v230\n"
    "v_lshlrev_b32 v246, 16, v231\n"
    "v_and_b32 v241, 0xffff0000, v228\n"
    "v_and_b32 v243, 0xffff0000, v229\n"
    "v_and_b32 v245, 0xffff0000, v230\n"
    "v_and_b32 v247, 0xffff0000, v231\n"
    "v_mul_f32 v240, s72, v240\n"
    "v_mul_f32 v242, s72, v242\n"
    "v_mul_f32 v244, s72, v244\n"
    "v_mul_f32 v246, s72, v246\n"
    "v_mul_f32 v241, s72, v241\n"
    "v_mul_f32 v243, s72, v243\n"
    "v_mul_f32 v245, s72, v245\n"
    "v_mul_f32 v247, s72, v247\n"
    "v_cvt_pk_bf16_f32 v228, v240, v241\n"
    "v_cvt_pk_bf16_f32 v229, v242, v243\n"
    "v_cvt_pk_bf16_f32 v230, v244, v245\n"
    "v_cvt_pk_bf16_f32 v231, v246, v247\n"
    "v_accvgpr_write_b32 a52, v228\n"
    "v_accvgpr_write_b32 a53, v229\n"
    "v_accvgpr_write_b32 a54, v230\n"
    "v_accvgpr_write_b32 a55, v231\n"
    "v_lshlrev_b32 v240, 16, v232\n"
    "v_lshlrev_b32 v242, 16, v233\n"
    "v_lshlrev_b32 v244, 16, v234\n"
    "v_lshlrev_b32 v246, 16, v235\n"
    "v_and_b32 v241, 0xffff0000, v232\n"
    "v_and_b32 v243, 0xffff0000, v233\n"
    "v_and_b32 v245, 0xffff0000, v234\n"
    "v_and_b32 v247, 0xffff0000, v235\n"
    "v_mul_f32 v240, s72, v240\n"
    "v_mul_f32 v242, s72, v242\n"
    "v_mul_f32 v244, s72, v244\n"
    "v_mul_f32 v246, s72, v246\n"
    "v_mul_f32 v241, s72, v241\n"
    "v_mul_f32 v243, s72, v243\n"
    "v_mul_f32 v245, s72, v245\n"
    "v_mul_f32 v247, s72, v247\n"
    "v_cvt_pk_bf16_f32 v232, v240, v241\n"
    "v_cvt_pk_bf16_f32 v233, v242, v243\n"
    "v_cvt_pk_bf16_f32 v234, v244, v245\n"
    "v_cvt_pk_bf16_f32 v235, v246, v247\n"
    "v_accvgpr_write_b32 a56, v232\n"
    "v_accvgpr_write_b32 a57, v233\n"
    "v_accvgpr_write_b32 a58, v234\n"
    "v_accvgpr_write_b32 a59, v235\n"
    "v_lshlrev_b32 v240, 16, v236\n"
    "v_lshlrev_b32 v242, 16, v237\n"
    "v_lshlrev_b32 v244, 16, v238\n"
    "v_lshlrev_b32 v246, 16, v239\n"
    "v_and_b32 v241, 0xffff0000, v236\n"
    "v_and_b32 v243, 0xffff0000, v237\n"
    "v_and_b32 v245, 0xffff0000, v238\n"
    "v_and_b32 v247, 0xffff0000, v239\n"
    "v_mul_f32 v240, s72, v240\n"
    "v_mul_f32 v242, s72, v242\n"
    "v_mul_f32 v244, s72, v244\n"
    "v_mul_f32 v246, s72, v246\n"
    "v_mul_f32 v241, s72, v241\n"
    "v_mul_f32 v243, s72, v243\n"
    "v_mul_f32 v245, s72, v245\n"
    "v_mul_f32 v247, s72, v247\n"
    "v_cvt_pk_bf16_f32 v236, v240, v241\n"
    "v_cvt_pk_bf16_f32 v237, v242, v243\n"
    "v_cvt_pk_bf16_f32 v238, v244, v245\n"
    "v_cvt_pk_bf16_f32 v239, v246, v247\n"
    "v_accvgpr_write_b32 a60, v236\n"
    "v_accvgpr_write_b32 a61, v237\n"
    "v_accvgpr_write_b32 a62, v238\n"
    "v_accvgpr_write_b32 a63, v239\n"
    "v_mfma_f32_16x16x32_bf16 v[152:155], a[128:131], a[48:51], 0\n"
    "v_mfma_f32_16x16x32_bf16 v[152:155], a[132:135], a[52:55], v[152:155]\n"
    "v_mfma_f32_16x16x32_bf16 v[152:155], a[136:139], a[56:59], v[152:155]\n"
    "v_mfma_f32_16x16x32_bf16 v[152:155], a[140:143], a[60:63], v[152:155]\n"
    "v_mfma_f32_16x16x32_bf16 v[156:159], a[144:147], a[48:51], 0\n"
    "v_mfma_f32_16x16x32_bf16 v[156:159], a[148:151], a[52:55], v[156:159]\n"
    "v_mfma_f32_16x16x32_bf16 v[156:159], a[152:155], a[56:59], v[156:159]\n"
    "v_mfma_f32_16x16x32_bf16 v[156:159], a[156:159], a[60:63], v[156:159]\n"
    "v_max3_f32 v251, v152, v153, v154\n"
    "s_nop 6\n"
    "v_max3_f32 v251, v251, v155, v156\n"
    "v_max3_f32 v251, v251, v157, v158\n"
    "v_max_f32 v251, v251, v159\n"
    "v_mov_b32 v252, v251\n"
    "s_nop 1\n"
    "v_permlane32_swap_b32 v251, v252\n"
    "v_max_f32 v251, v251, v252\n"
    "v_mov_b32 v252, v251\n"
    "s_nop 1\n"
    "v_permlane16_swap_b32 v251, v252\n"
    "v_max_f32 v251, v251, v252\n"
    "v_sub_f32 v172, 0, v251\n"
    "v_sub_f32 v173, 0, v251\n"
    "v_sub_f32 v174, 0, v251\n"
    "v_sub_f32 v175, 0, v251\n"
    "v_sub_f32 v152, v152, v251\n"
    "v_sub_f32 v153, v153, v251\n"
    "v_sub_f32 v154, v154, v251\n"
    "v_sub_f32 v155, v155, v251\n"
    "v_sub_f32 v156, v156, v251\n"
    "v_sub_f32 v157, v157, v251\n"
    "v_sub_f32 v158, v158, v251\n"
    "v_sub_f32 v159, v159, v251\n"
    "v_exp_f32 v152, v152\n"
    "v_exp_f32 v153, v153\n"
    "v_exp_f32 v154, v154\n"
    "v_exp_f32 v155, v155\n"
    "v_cvt_pk_bf16_f32 v188, v152, v153\n"
    "v_exp_f32 v156, v156\n"
    "v_cvt_pk_bf16_f32 v189, v154, v155\n"
    "v_exp_f32 v157, v157\n"
    "s_nop 0\n"
    "v_cvt_pk_bf16_f32 v190, v156, v157\n"
    "v_exp_f32 v158, v158\n"
    "v_exp_f32 v159, v159\n"
    "s_nop 0\n"
    "v_cvt_pk_bf16_f32 v191, v158, v159\n"
    "v_mfma_f32_16x16x32_bf16 v[152:155], a[64:67], a[48:51], v[172:175]\n"
    "v_mfma_f32_16x16x32_bf16 v[152:155], a[68:71], a[52:55], v[152:155]\n"
    "v_mfma_f32_16x16x32_bf16 v[152:155], a[72:75], a[56:59], v[152:155]\n"
    "v_mfma_f32_16x16x32_bf16 v[152:155], a[76:79], a[60:63], v[152:155]\n"
    "v_mfma_f32_16x16x32_bf16 v[156:159], a[80:83], a[48:51], v[172:175]\n"
    "v_mfma_f32_16x16x32_bf16 v[156:159], a[84:87], a[52:55], v[156:159]\n"
    "v_mfma_f32_16x16x32_bf16 v[156:159], a[88:91], a[56:59], v[156:159]\n"
    "v_mfma_f32_16x16x32_bf16 v[156:159], a[92:95], a[60:63], v[156:159]\n"
    "s_xor_b32 s81, s81, 0x0\n"
    "s_xor_b32 s82, s82, 0x4000\n"
    "s_mov_b32 s80, 0x2000\n"
    "s_mov_b32 s83, 0\n"
    "s_waitcnt vmcnt(6)\n"
    "s_barrier\n"
    "ds_read_b64_tr_b16 a[96:97], v209 offset:0\n"
    "ds_read_b64_tr_b16 a[98:99], v209 offset:4096\n"
    "ds_read_b64_tr_b16 a[100:101], v210 offset:0\n"
    "ds_read_b64_tr_b16 a[102:103], v210 offset:4096\n"
    "ds_read_b64_tr_b16 a[104:105], v209 offset:512\n"
    "ds_read_b64_tr_b16 a[106:107], v209 offset:4608\n"
    "ds_read_b64_tr_b16 a[108:109], v210 offset:512\n"
    "ds_read_b64_tr_b16 a[110:111], v210 offset:4608\n"
    "ds_read_b64_tr_b16 a[112:113], v209 offset:1024\n"
    "ds_read_b64_tr_b16 a[114:115], v209 offset:5120\n"
    "ds_read_b64_tr_b16 a[116:117], v210 offset:1024\n"
    "ds_read_b64_tr_b16 a[118:119], v210 offset:5120\n"
    "ds_read_b64_tr_b16 a[120:121], v209 offset:1536\n"
    "ds_read_b64_tr_b16 a[122:123], v209 offset:5632\n"
    "ds_read_b64_tr_b16 a[124:125], v210 offset:1536\n"
    "ds_read_b64_tr_b16 a[126:127], v210 offset:5632\n"
    "v_xor_b32 v209, s80, v209\n"
    "v_xor_b32 v210, s80, v210\n"
    ".p2align 6\n"
    "Lmain:\n"
    "s_waitcnt lgkmcnt(0)\n"
    "v_mfma_f32_16x16x32_bf16 v[0:3], a[96:99], v[176:179], v[0:3]\n"
    "v_exp_f32 v138, v138\n"
    "v_mfma_f32_16x16x32_bf16 v[4:7], a[100:103], v[176:179], v[4:7]\n"
    "ds_read_b128 a[64:67], v208 offset:0\n"
    "s_add_u32 s83, s83, 1\n"
    "v_mfma_f32_16x16x32_bf16 v[8:11], a[104:107], v[176:179], v[8:11]\n"
    "v_exp_f32 v139, v139\n"
    "v_mfma_f32_16x16x32_bf16 v[12:15], a[108:111], v[176:179], v[12:15]\n"
    "ds_read_b128 a[68:71], v208 offset:512\n"
    "s_mov_b32 m0, s81\n"
    "v_mfma_f32_16x16x32_bf16 v[16:19], a[112:115], v[176:179], v[16:19]\n"
    "v_exp_f32 v140, v140\n"
    "v_mfma_f32_16x16x32_bf16 v[20:23], a[116:119], v[176:179], v[20:23]\n"
    "buffer_load_dwordx4 v213, s[44:47], s56 offen nt lds\n"
    "v_mfma_f32_16x16x32_bf16 v[24:27], a[120:123], v[176:179], v[24:27]\n"
    "v_exp_f32 v141, v141\n"
    "v_mfma_f32_16x16x32_bf16 v[28:31], a[124:127], v[176:179], v[28:31]\n"
    "ds_read_b128 a[72:75], v208 offset:1024\n"
    "v_mfma_f32_16x16x32_bf16 v[192:195], a[160:163], v[176:179], v[192:195]\n"
    "v_exp_f32 v142, v142\n"
    "v_mfma_f32_16x16x32_bf16 v[32:35], a[96:99], v[180:183], v[32:35]\n"
    "buffer_load_dwordx4 v213, s[44:47], 0 offen offset:1024 nt lds\n"
    "v_cvt_pk_bf16_f32 v176, v128, v129\n"
    "v_mfma_f32_16x16x32_bf16 v[36:39], a[100:103], v[180:183], v[36:39]\n"
    "v_exp_f32 v143, v143\n"
    "v_mfma_f32_16x16x32_bf16 v[40:43], a[104:107], v[180:183], v[40:43]\n"
    "ds_read_b128 a[76:79], v208 offset:1536\n"
    "s_mov_b32 m0, s82\n"
    "v_mfma_f32_16x16x32_bf16 v[44:47], a[108:111], v[180:183], v[44:47]\n"
    "ds_read_b128 a[80:83], v208 offset:4096\n"
    "v_cvt_pk_bf16_f32 v177, v130, v131\n"
    "v_mfma_f32_16x16x32_bf16 v[48:51], a[112:115], v[180:183], v[48:51]\n"
    "v_exp_f32 v144, v144\n"
    "v_mfma_f32_16x16x32_bf16 v[52:55], a[116:119], v[180:183], v[52:55]\n"
    "buffer_load_dwordx4 v211, s[48:51], s56 offen nt lds\n"
    "v_cvt_pk_bf16_f32 v178, v132, v133\n"
    "v_mfma_f32_16x16x32_bf16 v[56:59], a[120:123], v[180:183], v[56:59]\n"
    "v_exp_f32 v145, v145\n"
    "v_mfma_f32_16x16x32_bf16 v[60:63], a[124:127], v[180:183], v[60:63]\n"
    "ds_read_b128 a[84:87], v208 offset:4608\n"
    "v_cvt_pk_bf16_f32 v179, v134, v135\n"
    "v_mfma_f32_16x16x32_bf16 v[196:199], a[160:163], v[180:183], v[196:199]\n"
    "v_exp_f32 v146, v146\n"
    "v_mfma_f32_16x16x32_bf16 v[64:67], a[96:99], v[184:187], v[64:67]\n"
    "ds_read_b128 a[88:91], v208 offset:5120\n"
    "v_cvt_pk_bf16_f32 v180, v136, v137\n"
    "v_mfma_f32_16x16x32_bf16 v[68:71], a[100:103], v[184:187], v[68:71]\n"
    "v_exp_f32 v147, v147\n"
    "v_mfma_f32_16x16x32_bf16 v[72:75], a[104:107], v[184:187], v[72:75]\n"
    "buffer_load_dwordx4 v211, s[48:51], 0 offen offset:1024 nt lds\n"
    "v_cvt_pk_bf16_f32 v181, v138, v139\n"
    "v_mfma_f32_16x16x32_bf16 v[76:79], a[108:111], v[184:187], v[76:79]\n"
    "v_exp_f32 v148, v148\n"
    "v_mfma_f32_16x16x32_bf16 v[80:83], a[112:115], v[184:187], v[80:83]\n"
    "ds_read_b128 a[92:95], v208 offset:5632\n"
    "v_cvt_pk_bf16_f32 v182, v140, v141\n"
    "v_mfma_f32_16x16x32_bf16 v[84:87], a[116:119], v[184:187], v[84:87]\n"
    "v_exp_f32 v149, v149\n"
    "v_mfma_f32_16x16x32_bf16 v[88:91], a[120:123], v[184:187], v[88:91]\n"
    "v_cvt_pk_bf16_f32 v183, v142, v143\n"
    "v_add_u32 v211, 0x2000, v211\n"
    "v_mfma_f32_16x16x32_bf16 v[92:95], a[124:127], v[184:187], v[92:95]\n"
    "v_exp_f32 v150, v150\n"
    "v_mfma_f32_16x16x32_bf16 v[200:203], a[160:163], v[184:187], v[200:203]\n"
    "v_add_u32 v213, 0x2000, v213\n"
    "v_xor_b32 v208, s80, v208\n"
    "v_mfma_f32_16x16x32_bf16 v[96:99], a[96:99], v[188:191], v[96:99]\n"
    "v_exp_f32 v151, v151\n"
    "v_mfma_f32_16x16x32_bf16 v[100:103], a[100:103], v[188:191], v[100:103]\n"
    "v_cvt_pk_bf16_f32 v184, v144, v145\n"
    "v_cvt_pk_bf16_f32 v185, v146, v147\n"
    "v_mfma_f32_16x16x32_bf16 v[104:107], a[104:107], v[188:191], v[104:107]\n"
    "v_cvt_pk_bf16_f32 v186, v148, v149\n"
    "v_cvt_pk_bf16_f32 v187, v150, v151\n"
    "v_mfma_f32_16x16x32_bf16 v[108:111], a[108:111], v[188:191], v[108:111]\n"
    "v_exp_f32 v152, v152\n"
    "v_mfma_f32_16x16x32_bf16 v[112:115], a[112:115], v[188:191], v[112:115]\n"
    "s_xor_b32 s81, s81, s80\n"
    "v_mfma_f32_16x16x32_bf16 v[116:119], a[116:119], v[188:191], v[116:119]\n"
    "v_exp_f32 v153, v153\n"
    "v_mfma_f32_16x16x32_bf16 v[120:123], a[120:123], v[188:191], v[120:123]\n"
    "s_xor_b32 s80, s80, 0x6000\n"
    "v_mfma_f32_16x16x32_bf16 v[124:127], a[124:127], v[188:191], v[124:127]\n"
    "v_exp_f32 v154, v154\n"
    "v_mfma_f32_16x16x32_bf16 v[204:207], a[160:163], v[188:191], v[204:207]\n"
    "s_xor_b32 s82, s82, s80\n"
    "s_cmp_eq_u32 s83, 14\n"
    "s_cbranch_scc1 Ltail\n"
    "s_waitcnt vmcnt(6) lgkmcnt(0)\n"
    "s_barrier\n"
    "v_mfma_f32_16x16x32_bf16 v[128:131], a[64:67], a[0:3], v[160:163]\n"
    "ds_read_b64_tr_b16 a[126:127], v210 offset:5632\n"
    "v_exp_f32 v155, v155\n"
    "v_mfma_f32_16x16x32_bf16 v[128:131], a[68:71], a[4:7], v[128:131]\n"
    "ds_read_b64_tr_b16 a[96:97], v209 offset:0\n"
    "v_cvt_pk_bf16_f32 v188, v152, v153\n"
    "v_mfma_f32_16x16x32_bf16 v[128:131], a[72:75], a[8:11], v[128:131]\n"
    "v_exp_f32 v156, v156\n"
    "v_mfma_f32_16x16x32_bf16 v[128:131], a[76:79], a[12:15], v[128:131]\n"
    "ds_read_b64_tr_b16 a[98:99], v209 offset:4096\n"
    "v_cvt_pk_bf16_f32 v189, v154, v155\n"
    "v_mfma_f32_16x16x32_bf16 v[132:135], a[80:83], a[0:3], v[160:163]\n"
    "v_exp_f32 v157, v157\n"
    "v_mfma_f32_16x16x32_bf16 v[132:135], a[84:87], a[4:7], v[132:135]\n"
    "ds_read_b64_tr_b16 a[100:101], v210 offset:0\n"
    "v_cvt_pk_bf16_f32 v190, v156, v157\n"
    "v_mfma_f32_16x16x32_bf16 v[132:135], a[88:91], a[8:11], v[132:135]\n"
    "v_exp_f32 v158, v158\n"
    "v_mfma_f32_16x16x32_bf16 v[132:135], a[92:95], a[12:15], v[132:135]\n"
    "ds_read_b64_tr_b16 a[102:103], v210 offset:4096\n"
    "v_mfma_f32_16x16x32_bf16 v[136:139], a[64:67], a[16:19], v[164:167]\n"
    "v_exp_f32 v159, v159\n"
    "v_mfma_f32_16x16x32_bf16 v[136:139], a[68:71], a[20:23], v[136:139]\n"
    "ds_read_b64_tr_b16 a[104:105], v209 offset:512\n"
    "v_cvt_pk_bf16_f32 v191, v158, v159\n"
    "v_mfma_f32_16x16x32_bf16 v[136:139], a[72:75], a[24:27], v[136:139]\n"
    "ds_read_b64_tr_b16 a[106:107], v209 offset:4608\n"
    "v_mfma_f32_16x16x32_bf16 v[136:139], a[76:79], a[28:31], v[136:139]\n"
    "v_exp_f32 v128, v128\n"
    "v_mfma_f32_16x16x32_bf16 v[140:143], a[80:83], a[16:19], v[164:167]\n"
    "ds_read_b64_tr_b16 a[108:109], v210 offset:512\n"
    "v_mfma_f32_16x16x32_bf16 v[140:143], a[84:87], a[20:23], v[140:143]\n"
    "v_exp_f32 v129, v129\n"
    "v_mfma_f32_16x16x32_bf16 v[140:143], a[88:91], a[24:27], v[140:143]\n"
    "ds_read_b64_tr_b16 a[110:111], v210 offset:4608\n"
    "v_mfma_f32_16x16x32_bf16 v[140:143], a[92:95], a[28:31], v[140:143]\n"
    "v_exp_f32 v130, v130\n"
    "v_mfma_f32_16x16x32_bf16 v[144:147], a[64:67], a[32:35], v[168:171]\n"
    "ds_read_b64_tr_b16 a[112:113], v209 offset:1024\n"
    "v_mfma_f32_16x16x32_bf16 v[144:147], a[68:71], a[36:39], v[144:147]\n"
    "v_exp_f32 v131, v131\n"
    "v_mfma_f32_16x16x32_bf16 v[144:147], a[72:75], a[40:43], v[144:147]\n"
    "ds_read_b64_tr_b16 a[114:115], v209 offset:5120\n"
    "v_mfma_f32_16x16x32_bf16 v[144:147], a[76:79], a[44:47], v[144:147]\n"
    "v_exp_f32 v132, v132\n"
    "v_mfma_f32_16x16x32_bf16 v[148:151], a[80:83], a[32:35], v[168:171]\n"
    "ds_read_b64_tr_b16 a[116:117], v210 offset:1024\n"
    "v_mfma_f32_16x16x32_bf16 v[148:151], a[84:87], a[36:39], v[148:151]\n"
    "v_exp_f32 v133, v133\n"
    "v_mfma_f32_16x16x32_bf16 v[148:151], a[88:91], a[40:43], v[148:151]\n"
    "ds_read_b64_tr_b16 a[118:119], v210 offset:5120\n"
    "v_mfma_f32_16x16x32_bf16 v[148:151], a[92:95], a[44:47], v[148:151]\n"
    "v_exp_f32 v134, v134\n"
    "v_mfma_f32_16x16x32_bf16 v[152:155], a[64:67], a[48:51], v[172:175]\n"
    "ds_read_b64_tr_b16 a[120:121], v209 offset:1536\n"
    "v_mfma_f32_16x16x32_bf16 v[152:155], a[68:71], a[52:55], v[152:155]\n"
    "v_exp_f32 v135, v135\n"
    "v_mfma_f32_16x16x32_bf16 v[152:155], a[72:75], a[56:59], v[152:155]\n"
    "ds_read_b64_tr_b16 a[122:123], v209 offset:5632\n"
    "v_mfma_f32_16x16x32_bf16 v[152:155], a[76:79], a[60:63], v[152:155]\n"
    "ds_read_b64_tr_b16 a[124:125], v210 offset:1536\n"
    "v_xor_b32 v209, s80, v209\n"
    "v_mfma_f32_16x16x32_bf16 v[156:159], a[80:83], a[48:51], v[172:175]\n"
    "v_exp_f32 v136, v136\n"
    "v_mfma_f32_16x16x32_bf16 v[156:159], a[84:87], a[52:55], v[156:159]\n"
    "v_xor_b32 v210, s80, v210\n"
    "v_mfma_f32_16x16x32_bf16 v[156:159], a[88:91], a[56:59], v[156:159]\n"
    "v_exp_f32 v137, v137\n"
    "v_mfma_f32_16x16x32_bf16 v[156:159], a[92:95], a[60:63], v[156:159]\n"
    "s_branch Lmain\n"
    "Ltail:\n"
    "s_waitcnt vmcnt(8) lgkmcnt(0)\n"
    "s_barrier\n"
    "v_mfma_f32_16x16x32_bf16 v[128:131], a[64:67], a[0:3], v[160:163]\n"
    "ds_read_b64_tr_b16 a[96:97], v209 offset:0\n"
    "ds_read_b64_tr_b16 a[98:99], v209 offset:4096\n"
    "v_mfma_f32_16x16x32_bf16 v[128:131], a[68:71], a[4:7], v[128:131]\n"
    "ds_read_b64_tr_b16 a[100:101], v210 offset:0\n"
    "ds_read_b64_tr_b16 a[102:103], v210 offset:4096\n"
    "v_mfma_f32_16x16x32_bf16 v[128:131], a[72:75], a[8:11], v[128:131]\n"
    "ds_read_b64_tr_b16 a[104:105], v209 offset:512\n"
    "ds_read_b64_tr_b16 a[106:107], v209 offset:4608\n"
    "v_mfma_f32_16x16x32_bf16 v[128:131], a[76:79], a[12:15], v[128:131]\n"
    "ds_read_b64_tr_b16 a[108:109], v210 offset:512\n"
    "ds_read_b64_tr_b16 a[110:111], v210 offset:4608\n"
    "v_mfma_f32_16x16x32_bf16 v[132:135], a[80:83], a[0:3], v[160:163]\n"
    "ds_read_b64_tr_b16 a[112:113], v209 offset:1024\n"
    "ds_read_b64_tr_b16 a[114:115], v209 offset:5120\n"
    "v_mfma_f32_16x16x32_bf16 v[132:135], a[84:87], a[4:7], v[132:135]\n"
    "ds_read_b64_tr_b16 a[116:117], v210 offset:1024\n"
    "ds_read_b64_tr_b16 a[118:119], v210 offset:5120\n"
    "v_mfma_f32_16x16x32_bf16 v[132:135], a[88:91], a[8:11], v[132:135]\n"
    "ds_read_b64_tr_b16 a[120:121], v209 offset:1536\n"
    "ds_read_b64_tr_b16 a[122:123], v209 offset:5632\n"
    "v_mfma_f32_16x16x32_bf16 v[132:135], a[92:95], a[12:15], v[132:135]\n"
    "ds_read_b64_tr_b16 a[124:125], v210 offset:1536\n"
    "ds_read_b64_tr_b16 a[126:127], v210 offset:5632\n"
    "v_mfma_f32_16x16x32_bf16 v[192:195], a[160:163], v[176:179], v[192:195]\n"
    "v_mbcnt_lo_u32_b32 v230, -1, 0\n"
    "v_mbcnt_hi_u32_b32 v230, -1, v230\n"
    "s_waitcnt vmcnt(4)\n"
    "s_barrier\n"
    "s_cmp_lt_u32 s65, 1\n"
    "s_cbranch_scc1 Lstag\n"
    "s_nop 3\n"
    "s_nop 3\n"
    "s_nop 3\n"
    "s_cmp_lt_u32 s65, 2\n"
    "s_cbranch_scc1 Lstag\n"
    "s_nop 3\n"
    "s_nop 3\n"
    "s_nop 3\n"
    "s_cmp_lt_u32 s65, 3\n"
    "s_cbranch_scc1 Lstag\n"
    "s_nop 3\n"
    "s_nop 3\n"
    "s_nop 3\n"
    "Lstag:\n"
    "v_mov_b32 v222, v209\n"
    "v_mov_b32 v223, v210\n"
    "v_xor_b32 v222, s80, v222\n"
    "v_xor_b32 v223, s80, v223\n"
    "s_waitcnt lgkmcnt(1)\n"
    "v_mfma_f32_16x16x32_bf16 v[0:3], a[96:99], v[176:179], v[0:3]\n"
    "ds_read_b64_tr_b16 a[128:129], v222 offset:0\n"
    "ds_read_b64_tr_b16 a[130:131], v222 offset:4096\n"
    "v_mfma_f32_16x16x32_bf16 v[4:7], a[100:103], v[176:179], v[4:7]\n"
    "ds_read_b64_tr_b16 a[132:133], v223 offset:0\n"
    "ds_read_b64_tr_b16 a[134:135], v223 offset:4096\n"
    "v_mfma_f32_16x16x32_bf16 v[8:11], a[104:107], v[176:179], v[8:11]\n"
    "ds_read_b64_tr_b16 a[136:137], v222 offset:512\n"
    "ds_read_b64_tr_b16 a[138:139], v222 offset:4608\n"
    "v_mfma_f32_16x16x32_bf16 v[12:15], a[108:111], v[176:179], v[12:15]\n"
    "ds_read_b64_tr_b16 a[140:141], v223 offset:512\n"
    "ds_read_b64_tr_b16 a[142:143], v223 offset:4608\n"
    "v_mfma_f32_16x16x32_bf16 v[16:19], a[112:115], v[176:179], v[16:19]\n"
    "v_exp_f32 v128, v128\n"
    "v_mfma_f32_16x16x32_bf16 v[20:23], a[116:119], v[176:179], v[20:23]\n"
    "v_exp_f32 v129, v129\n"
    "v_mfma_f32_16x16x32_bf16 v[24:27], a[120:123], v[176:179], v[24:27]\n"
    "v_exp_f32 v130, v130\n"
    "s_waitcnt lgkmcnt(8)\n"
    "v_mfma_f32_16x16x32_bf16 v[28:31], a[124:127], v[176:179], v[28:31]\n"
    "v_exp_f32 v131, v131\n"
    "v_mfma_f32_16x16x32_bf16 v[136:139], a[64:67], a[16:19], v[164:167]\n"
    "v_exp_f32 v132, v132\n"
    "v_mfma_f32_16x16x32_bf16 v[136:139], a[68:71], a[20:23], v[136:139]\n"
    "v_exp_f32 v133, v133\n"
    "v_mfma_f32_16x16x32_bf16 v[136:139], a[72:75], a[24:27], v[136:139]\n"
    "v_exp_f32 v134, v134\n"
    "v_mfma_f32_16x16x32_bf16 v[136:139], a[76:79], a[28:31], v[136:139]\n"
    "v_exp_f32 v135, v135\n"
    "v_mfma_f32_16x16x32_bf16 v[140:143], a[80:83], a[16:19], v[164:167]\n"
    "ds_read_b64_tr_b16 a[144:145], v222 offset:1024\n"
    "ds_read_b64_tr_b16 a[146:147], v222 offset:5120\n"
    "v_mfma_f32_16x16x32_bf16 v[140:143], a[84:87], a[20:23], v[140:143]\n"
    "v_cvt_pk_bf16_f32 v176, v128, v129\n"
    "v_cvt_pk_bf16_f32 v177, v130, v131\n"
    "v_mfma_f32_16x16x32_bf16 v[140:143], a[88:91], a[24:27], v[140:143]\n"
    "v_cvt_pk_bf16_f32 v178, v132, v133\n"
    "v_cvt_pk_bf16_f32 v179, v134, v135\n"
    "v_mfma_f32_16x16x32_bf16 v[140:143], a[92:95], a[28:31], v[140:143]\n"
    "ds_read_b64_tr_b16 a[148:149], v223 offset:1024\n"
    "ds_read_b64_tr_b16 a[150:151], v223 offset:5120\n"
    "v_mfma_f32_16x16x32_bf16 v[192:195], a[160:163], v[176:179], v[192:195]\n"
    "ds_read_b64_tr_b16 a[152:153], v222 offset:1536\n"
    "ds_read_b64_tr_b16 a[154:155], v222 offset:5632\n"
    "s_waitcnt lgkmcnt(6)\n"
    "v_mfma_f32_16x16x32_bf16 v[0:3], a[128:131], v[176:179], v[0:3]\n"
    "ds_read_b64_tr_b16 a[156:157], v223 offset:1536\n"
    "ds_read_b64_tr_b16 a[158:159], v223 offset:5632\n"
    "v_mfma_f32_16x16x32_bf16 v[4:7], a[132:135], v[176:179], v[4:7]\n"
    "s_lshl_b32 s59, s65, 14\n"
    "v_and_b32 v233, 15, v230\n"
    "v_mfma_f32_16x16x32_bf16 v[8:11], a[136:139], v[176:179], v[8:11]\n"
    "v_bfe_u32 v231, v230, 4, 1\n"
    "v_lshrrev_b32 v232, 5, v230\n"
    "v_mfma_f32_16x16x32_bf16 v[12:15], a[140:143], v[176:179], v[12:15]\n"
    "v_lshl_add_u32 v231, v231, 1, v232\n"
    "v_and_b32 v232, 7, v230\n"
    "s_waitcnt lgkmcnt(6)\n"
    "v_mfma_f32_16x16x32_bf16 v[16:19], a[144:147], v[176:179], v[16:19]\n"
    "v_xor_b32 v231, v231, v232\n"
    "v_lshlrev_b32 v231, 4, v231\n"
    "s_waitcnt lgkmcnt(4)\n"
    "v_mfma_f32_16x16x32_bf16 v[20:23], a[148:151], v[176:179], v[20:23]\n"
    "v_lshl_add_u32 v231, v233, 8, v231\n"
    "v_add_u32 v226, s59, v231\n"
    "s_waitcnt lgkmcnt(2)\n"
    "v_mfma_f32_16x16x32_bf16 v[24:27], a[152:155], v[176:179], v[24:27]\n"
    "v_xor_b32 v227, 64, v226\n"
    "v_lshrrev_b32 v233, 4, v230\n"
    "s_waitcnt lgkmcnt(0)\n"
    "v_mfma_f32_16x16x32_bf16 v[28:31], a[156:159], v[176:179], v[28:31]\n"
    "v_and_b32 v231, 15, v230\n"
    "v_xor_b32 v231, v231, v233\n"
    "v_lshlrev_b32 v231, 4, v231\n"
    "v_lshl_add_u32 v231, v233, 8, v231\n"
    "v_add_u32 v228, s59, v231\n"
    "v_xor_b32 v229, 64, v228\n"
    "v_lshlrev_b32 v212, 4, v230\n"
    "s_mov_b32 s67, 16384\n"
    "s_mov_b32 s68, 32768\n"
    "s_mov_b32 s69, 49152\n"
    "v_mfma_f32_16x16x32_bf16 v[196:199], a[160:163], v[180:183], v[196:199]\n"
    "v_exp_f32 v136, v136\n"
    "v_mfma_f32_16x16x32_bf16 v[32:35], a[96:99], v[180:183], v[32:35]\n"
    "v_exp_f32 v137, v137\n"
    "v_mfma_f32_16x16x32_bf16 v[36:39], a[100:103], v[180:183], v[36:39]\n"
    "v_rcp_f32 v214, v192\n"
    "v_mfma_f32_16x16x32_bf16 v[40:43], a[104:107], v[180:183], v[40:43]\n"
    "v_pk_mul_f32 v[0:1], v[0:1], v[214:215] op_sel_hi:[1,0]\n"
    "v_pk_mul_f32 v[2:3], v[2:3], v[214:215] op_sel_hi:[1,0]\n"
    "v_mfma_f32_16x16x32_bf16 v[44:47], a[108:111], v[180:183], v[44:47]\n"
    "v_pk_mul_f32 v[4:5], v[4:5], v[214:215] op_sel_hi:[1,0]\n"
    "v_pk_mul_f32 v[6:7], v[6:7], v[214:215] op_sel_hi:[1,0]\n"
    "v_mfma_f32_16x16x32_bf16 v[48:51], a[112:115], v[180:183], v[48:51]\n"
    "v_pk_mul_f32 v[8:9], v[8:9], v[214:215] op_sel_hi:[1,0]\n"
    "v_pk_mul_f32 v[10:11], v[10:11], v[214:215] op_sel_hi:[1,0]\n"
    "v_mfma_f32_16x16x32_bf16 v[52:55], a[116:119], v[180:183], v[52:55]\n"
    "v_pk_mul_f32 v[12:13], v[12:13], v[214:215] op_sel_hi:[1,0]\n"
    "v_pk_mul_f32 v[14:15], v[14:15], v[214:215] op_sel_hi:[1,0]\n"
    "v_mfma_f32_16x16x32_bf16 v[56:59], a[120:123], v[180:183], v[56:59]\n"
    "v_pk_mul_f32 v[16:17], v[16:17], v[214:215] op_sel_hi:[1,0]\n"
    "v_pk_mul_f32 v[18:19], v[18:19], v[214:215] op_sel_hi:[1,0]\n"
    "v_mfma_f32_16x16x32_bf16 v[60:63], a[124:127], v[180:183], v[60:63]\n"
    "v_pk_mul_f32 v[20:21], v[20:21], v[214:215] op_sel_hi:[1,0]\n"
    "v_pk_mul_f32 v[22:23], v[22:23], v[214:215] op_sel_hi:[1,0]\n"
    "v_pk_mul_f32 v[24:25], v[24:25], v[214:215] op_sel_hi:[1,0]\n"
    "v_pk_mul_f32 v[26:27], v[26:27], v[214:215] op_sel_hi:[1,0]\n"
    "v_pk_mul_f32 v[28:29], v[28:29], v[214:215] op_sel_hi:[1,0]\n"
    "v_pk_mul_f32 v[30:31], v[30:31], v[214:215] op_sel_hi:[1,0]\n"
    "v_cvt_pk_bf16_f32 v0, v0, v1\n"
    "v_cvt_pk_bf16_f32 v1, v2, v3\n"
    "v_cvt_pk_bf16_f32 v2, v4, v5\n"
    "v_cvt_pk_bf16_f32 v3, v6, v7\n"
    "v_cvt_pk_bf16_f32 v8, v8, v9\n"
    "v_cvt_pk_bf16_f32 v9, v10, v11\n"
    "v_cvt_pk_bf16_f32 v10, v12, v13\n"
    "v_cvt_pk_bf16_f32 v11, v14, v15\n"
    "v_cvt_pk_bf16_f32 v16, v16, v17\n"
    "v_cvt_pk_bf16_f32 v17, v18, v19\n"
    "v_cvt_pk_bf16_f32 v18, v20, v21\n"
    "v_cvt_pk_bf16_f32 v19, v22, v23\n"
    "v_cvt_pk_bf16_f32 v24, v24, v25\n"
    "v_cvt_pk_bf16_f32 v25, v26, v27\n"
    "v_cvt_pk_bf16_f32 v26, v28, v29\n"
    "v_cvt_pk_bf16_f32 v27, v30, v31\n"
    "v_permlane16_swap_b32 v0, v2\n"
    "v_permlane16_swap_b32 v1, v3\n"
    "v_permlane16_swap_b32 v8, v10\n"
    "v_permlane16_swap_b32 v9, v11\n"
    "v_permlane16_swap_b32 v16, v18\n"
    "v_permlane16_swap_b32 v17, v19\n"
    "v_permlane16_swap_b32 v24, v26\n"
    "v_permlane16_swap_b32 v25, v27\n"
    "ds_write_b128 v226, v[0:3] offset:0\n"
    "ds_write_b128 v227, v[8:11] offset:0\n"
    "ds_write_b128 v226, v[16:19] offset:128\n"
    "ds_write_b128 v227, v[24:27] offset:128\n"
    "ds_read_b128 v[4:7], v228 offset:0\n"
    "ds_read_b128 v[12:15], v229 offset:1024\n"
    "ds_read_b128 v[20:23], v228 offset:2048\n"
    "ds_read_b128 v[28:31], v229 offset:3072\n"
    "v_mfma_f32_16x16x32_bf16 v[144:147], a[64:67], a[32:35], v[168:171]\n"
    "v_exp_f32 v138, v138\n"
    "v_mfma_f32_16x16x32_bf16 v[144:147], a[68:71], a[36:39], v[144:147]\n"
    "v_exp_f32 v139, v139\n"
    "v_mfma_f32_16x16x32_bf16 v[144:147], a[72:75], a[40:43], v[144:147]\n"
    "v_exp_f32 v140, v140\n"
    "v_mfma_f32_16x16x32_bf16 v[144:147], a[76:79], a[44:47], v[144:147]\n"
    "s_waitcnt lgkmcnt(3)\n"
    "buffer_store_dwordx4 v[4:7], v212, s[52:55], 0 offen sc0 sc1 nt\n"
    "s_waitcnt lgkmcnt(2)\n"
    "buffer_store_dwordx4 v[12:15], v212, s[52:55], 0 offen offset:1024 sc0 sc1 nt\n"
    "s_waitcnt lgkmcnt(1)\n"
    "buffer_store_dwordx4 v[20:23], v212, s[52:55], 0 offen offset:2048 sc0 sc1 nt\n"
    "s_waitcnt lgkmcnt(0)\n"
    "buffer_store_dwordx4 v[28:31], v212, s[52:55], 0 offen offset:3072 sc0 sc1 nt\n"
    "v_mfma_f32_16x16x32_bf16 v[148:151], a[80:83], a[32:35], v[168:171]\n"
    "v_exp_f32 v141, v141\n"
    "v_mfma_f32_16x16x32_bf16 v[148:151], a[84:87], a[36:39], v[148:151]\n"
    "v_exp_f32 v142, v142\n"
    "v_exp_f32 v143, v143\n"
    "v_mfma_f32_16x16x32_bf16 v[148:151], a[88:91], a[40:43], v[148:151]\n"
    "v_cvt_pk_bf16_f32 v180, v136, v137\n"
    "v_cvt_pk_bf16_f32 v181, v138, v139\n"
    "v_cvt_pk_bf16_f32 v182, v140, v141\n"
    "v_cvt_pk_bf16_f32 v183, v142, v143\n"
    "v_mfma_f32_16x16x32_bf16 v[148:151], a[92:95], a[44:47], v[148:151]\n"
    "v_exp_f32 v155, v155\n"
    "v_mfma_f32_16x16x32_bf16 v[196:199], a[160:163], v[180:183], v[196:199]\n"
    "v_exp_f32 v156, v156\n"
    "v_mfma_f32_16x16x32_bf16 v[32:35], a[128:131], v[180:183], v[32:35]\n"
    "v_exp_f32 v157, v157\n"
    "v_mfma_f32_16x16x32_bf16 v[36:39], a[132:135], v[180:183], v[36:39]\n"
    "v_exp_f32 v158, v158\n"
    "v_mfma_f32_16x16x32_bf16 v[40:43], a[136:139], v[180:183], v[40:43]\n"
    "v_exp_f32 v159, v159\n"
    "v_mfma_f32_16x16x32_bf16 v[44:47], a[140:143], v[180:183], v[44:47]\n"
    "v_cvt_pk_bf16_f32 v188, v152, v153\n"
    "v_cvt_pk_bf16_f32 v189, v154, v155\n"
    "v_mfma_f32_16x16x32_bf16 v[48:51], a[144:147], v[180:183], v[48:51]\n"
    "v_cvt_pk_bf16_f32 v190, v156, v157\n"
    "v_cvt_pk_bf16_f32 v191, v158, v159\n"
    "v_mfma_f32_16x16x32_bf16 v[52:55], a[148:151], v[180:183], v[52:55]\n"
    "v_exp_f32 v144, v144\n"
    "v_mfma_f32_16x16x32_bf16 v[56:59], a[152:155], v[180:183], v[56:59]\n"
    "v_exp_f32 v145, v145\n"
    "v_mfma_f32_16x16x32_bf16 v[60:63], a[156:159], v[180:183], v[60:63]\n"
    "v_exp_f32 v146, v146\n"
    "v_mfma_f32_16x16x32_bf16 v[200:203], a[160:163], v[184:187], v[200:203]\n"
    "v_exp_f32 v147, v147\n"
    "v_mfma_f32_16x16x32_bf16 v[64:67], a[96:99], v[184:187], v[64:67]\n"
    "v_exp_f32 v148, v148\n"
    "v_mfma_f32_16x16x32_bf16 v[68:71], a[100:103], v[184:187], v[68:71]\n"
    "v_rcp_f32 v216, v196\n"
    "v_mfma_f32_16x16x32_bf16 v[72:75], a[104:107], v[184:187], v[72:75]\n"
    "v_pk_mul_f32 v[32:33], v[32:33], v[216:217] op_sel_hi:[1,0]\n"
    "v_pk_mul_f32 v[34:35], v[34:35], v[216:217] op_sel_hi:[1,0]\n"
    "v_mfma_f32_16x16x32_bf16 v[76:79], a[108:111], v[184:187], v[76:79]\n"
    "v_pk_mul_f32 v[36:37], v[36:37], v[216:217] op_sel_hi:[1,0]\n"
    "v_pk_mul_f32 v[38:39], v[38:39], v[216:217] op_sel_hi:[1,0]\n"
    "v_mfma_f32_16x16x32_bf16 v[80:83], a[112:115], v[184:187], v[80:83]\n"
    "v_pk_mul_f32 v[40:41], v[40:41], v[216:217] op_sel_hi:[1,0]\n"
    "v_pk_mul_f32 v[42:43], v[42:43], v[216:217] op_sel_hi:[1,0]\n"
    "v_mfma_f32_16x16x32_bf16 v[84:87], a[116:119], v[184:187], v[84:87]\n"
    "v_pk_mul_f32 v[44:45], v[44:45], v[216:217] op_sel_hi:[1,0]\n"
    "v_pk_mul_f32 v[46:47], v[46:47], v[216:217] op_sel_hi:[1,0]\n"
    "v_mfma_f32_16x16x32_bf16 v[88:91], a[120:123], v[184:187], v[88:91]\n"
    "v_pk_mul_f32 v[48:49], v[48:49], v[216:217] op_sel_hi:[1,0]\n"
    "v_pk_mul_f32 v[50:51], v[50:51], v[216:217] op_sel_hi:[1,0]\n"
    "v_mfma_f32_16x16x32_bf16 v[92:95], a[124:127], v[184:187], v[92:95]\n"
    "v_pk_mul_f32 v[52:53], v[52:53], v[216:217] op_sel_hi:[1,0]\n"
    "v_pk_mul_f32 v[54:55], v[54:55], v[216:217] op_sel_hi:[1,0]\n"
    "v_pk_mul_f32 v[56:57], v[56:57], v[216:217] op_sel_hi:[1,0]\n"
    "v_pk_mul_f32 v[58:59], v[58:59], v[216:217] op_sel_hi:[1,0]\n"
    "v_pk_mul_f32 v[60:61], v[60:61], v[216:217] op_sel_hi:[1,0]\n"
    "v_pk_mul_f32 v[62:63], v[62:63], v[216:217] op_sel_hi:[1,0]\n"
    "v_cvt_pk_bf16_f32 v32, v32, v33\n"
    "v_cvt_pk_bf16_f32 v33, v34, v35\n"
    "v_cvt_pk_bf16_f32 v34, v36, v37\n"
    "v_cvt_pk_bf16_f32 v35, v38, v39\n"
    "v_cvt_pk_bf16_f32 v40, v40, v41\n"
    "v_cvt_pk_bf16_f32 v41, v42, v43\n"
    "v_cvt_pk_bf16_f32 v42, v44, v45\n"
    "v_cvt_pk_bf16_f32 v43, v46, v47\n"
    "v_cvt_pk_bf16_f32 v48, v48, v49\n"
    "v_cvt_pk_bf16_f32 v49, v50, v51\n"
    "v_cvt_pk_bf16_f32 v50, v52, v53\n"
    "v_cvt_pk_bf16_f32 v51, v54, v55\n"
    "v_cvt_pk_bf16_f32 v56, v56, v57\n"
    "v_cvt_pk_bf16_f32 v57, v58, v59\n"
    "v_cvt_pk_bf16_f32 v58, v60, v61\n"
    "v_cvt_pk_bf16_f32 v59, v62, v63\n"
    "v_permlane16_swap_b32 v32, v34\n"
    "v_permlane16_swap_b32 v33, v35\n"
    "v_permlane16_swap_b32 v40, v42\n"
    "v_permlane16_swap_b32 v41, v43\n"
    "v_permlane16_swap_b32 v48, v50\n"
    "v_permlane16_swap_b32 v49, v51\n"
    "v_permlane16_swap_b32 v56, v58\n"
    "v_permlane16_swap_b32 v57, v59\n"
    "ds_write_b128 v226, v[32:35] offset:4096\n"
    "ds_write_b128 v227, v[40:43] offset:4096\n"
    "ds_write_b128 v226, v[48:51] offset:4224\n"
    "ds_write_b128 v227, v[56:59] offset:4224\n"
    "ds_read_b128 v[36:39], v228 offset:4096\n"
    "ds_read_b128 v[44:47], v229 offset:5120\n"
    "ds_read_b128 v[52:55], v228 offset:6144\n"
    "ds_read_b128 v[60:63], v229 offset:7168\n"
    "v_mfma_f32_16x16x32_bf16 v[152:155], a[64:67], a[48:51], v[172:175]\n"
    "v_exp_f32 v149, v149\n"
    "v_mfma_f32_16x16x32_bf16 v[152:155], a[68:71], a[52:55], v[152:155]\n"
    "v_exp_f32 v150, v150\n"
    "v_mfma_f32_16x16x32_bf16 v[152:155], a[72:75], a[56:59], v[152:155]\n"
    "v_exp_f32 v151, v151\n"
    "v_mfma_f32_16x16x32_bf16 v[152:155], a[76:79], a[60:63], v[152:155]\n"
    "s_waitcnt lgkmcnt(3)\n"
    "buffer_store_dwordx4 v[36:39], v212, s[52:55], s67 offen sc0 sc1 nt\n"
    "s_waitcnt lgkmcnt(2)\n"
    "buffer_store_dwordx4 v[44:47], v212, s[52:55], s67 offen offset:1024 sc0 sc1 nt\n"
    "s_waitcnt lgkmcnt(1)\n"
    "buffer_store_dwordx4 v[52:55], v212, s[52:55], s67 offen offset:2048 sc0 sc1 nt\n"
    "s_waitcnt lgkmcnt(0)\n"
    "buffer_store_dwordx4 v[60:63], v212, s[52:55], s67 offen offset:3072 sc0 sc1 nt\n"
    "v_mfma_f32_16x16x32_bf16 v[156:159], a[80:83], a[48:51], v[172:175]\n"
    "v_cvt_pk_bf16_f32 v184, v144, v145\n"
    "v_cvt_pk_bf16_f32 v185, v146, v147\n"
    "v_mfma_f32_16x16x32_bf16 v[156:159], a[84:87], a[52:55], v[156:159]\n"
    "v_cvt_pk_bf16_f32 v186, v148, v149\n"
    "v_cvt_pk_bf16_f32 v187, v150, v151\n"
    "v_mfma_f32_16x16x32_bf16 v[156:159], a[88:91], a[56:59], v[156:159]\n"
    "v_mfma_f32_16x16x32_bf16 v[156:159], a[92:95], a[60:63], v[156:159]\n"
    "v_mfma_f32_16x16x32_bf16 v[204:207], a[160:163], v[188:191], v[204:207]\n"
    "v_mfma_f32_16x16x32_bf16 v[96:99], a[96:99], v[188:191], v[96:99]\n"
    "v_mfma_f32_16x16x32_bf16 v[100:103], a[100:103], v[188:191], v[100:103]\n"
    "v_exp_f32 v152, v152\n"
    "v_mfma_f32_16x16x32_bf16 v[104:107], a[104:107], v[188:191], v[104:107]\n"
    "v_exp_f32 v153, v153\n"
    "v_mfma_f32_16x16x32_bf16 v[108:111], a[108:111], v[188:191], v[108:111]\n"
    "v_exp_f32 v154, v154\n"
    "v_mfma_f32_16x16x32_bf16 v[112:115], a[112:115], v[188:191], v[112:115]\n"
    "v_exp_f32 v155, v155\n"
    "v_mfma_f32_16x16x32_bf16 v[116:119], a[116:119], v[188:191], v[116:119]\n"
    "v_exp_f32 v156, v156\n"
    "v_mfma_f32_16x16x32_bf16 v[120:123], a[120:123], v[188:191], v[120:123]\n"
    "v_exp_f32 v157, v157\n"
    "v_mfma_f32_16x16x32_bf16 v[124:127], a[124:127], v[188:191], v[124:127]\n"
    "v_exp_f32 v158, v158\n"
    "v_mfma_f32_16x16x32_bf16 v[200:203], a[160:163], v[184:187], v[200:203]\n"
    "v_exp_f32 v159, v159\n"
    "v_mfma_f32_16x16x32_bf16 v[64:67], a[128:131], v[184:187], v[64:67]\n"
    "v_cvt_pk_bf16_f32 v188, v152, v153\n"
    "v_cvt_pk_bf16_f32 v189, v154, v155\n"
    "v_mfma_f32_16x16x32_bf16 v[68:71], a[132:135], v[184:187], v[68:71]\n"
    "v_cvt_pk_bf16_f32 v190, v156, v157\n"
    "v_cvt_pk_bf16_f32 v191, v158, v159\n"
    "v_mfma_f32_16x16x32_bf16 v[72:75], a[136:139], v[184:187], v[72:75]\n"
    "v_mfma_f32_16x16x32_bf16 v[76:79], a[140:143], v[184:187], v[76:79]\n"
    "v_mfma_f32_16x16x32_bf16 v[80:83], a[144:147], v[184:187], v[80:83]\n"
    "v_mfma_f32_16x16x32_bf16 v[84:87], a[148:151], v[184:187], v[84:87]\n"
    "v_mfma_f32_16x16x32_bf16 v[88:91], a[152:155], v[184:187], v[88:91]\n"
    "v_mfma_f32_16x16x32_bf16 v[92:95], a[156:159], v[184:187], v[92:95]\n"
    "v_mfma_f32_16x16x32_bf16 v[204:207], a[160:163], v[188:191], v[204:207]\n"
    "v_mfma_f32_16x16x32_bf16 v[96:99], a[128:131], v[188:191], v[96:99]\n"
    "v_mfma_f32_16x16x32_bf16 v[100:103], a[132:135], v[188:191], v[100:103]\n"
    "v_rcp_f32 v218, v200\n"
    "v_mfma_f32_16x16x32_bf16 v[104:107], a[136:139], v[188:191], v[104:107]\n"
    "v_pk_mul_f32 v[64:65], v[64:65], v[218:219] op_sel_hi:[1,0]\n"
    "v_pk_mul_f32 v[66:67], v[66:67], v[218:219] op_sel_hi:[1,0]\n"
    "v_mfma_f32_16x16x32_bf16 v[108:111], a[140:143], v[188:191], v[108:111]\n"
    "v_pk_mul_f32 v[68:69], v[68:69], v[218:219] op_sel_hi:[1,0]\n"
    "v_pk_mul_f32 v[70:71], v[70:71], v[218:219] op_sel_hi:[1,0]\n"
    "v_mfma_f32_16x16x32_bf16 v[112:115], a[144:147], v[188:191], v[112:115]\n"
    "v_pk_mul_f32 v[72:73], v[72:73], v[218:219] op_sel_hi:[1,0]\n"
    "v_pk_mul_f32 v[74:75], v[74:75], v[218:219] op_sel_hi:[1,0]\n"
    "v_mfma_f32_16x16x32_bf16 v[116:119], a[148:151], v[188:191], v[116:119]\n"
    "v_pk_mul_f32 v[76:77], v[76:77], v[218:219] op_sel_hi:[1,0]\n"
    "v_pk_mul_f32 v[78:79], v[78:79], v[218:219] op_sel_hi:[1,0]\n"
    "v_mfma_f32_16x16x32_bf16 v[120:123], a[152:155], v[188:191], v[120:123]\n"
    "v_pk_mul_f32 v[80:81], v[80:81], v[218:219] op_sel_hi:[1,0]\n"
    "v_pk_mul_f32 v[82:83], v[82:83], v[218:219] op_sel_hi:[1,0]\n"
    "v_mfma_f32_16x16x32_bf16 v[124:127], a[156:159], v[188:191], v[124:127]\n"
    "v_pk_mul_f32 v[84:85], v[84:85], v[218:219] op_sel_hi:[1,0]\n"
    "v_pk_mul_f32 v[86:87], v[86:87], v[218:219] op_sel_hi:[1,0]\n"
    "v_pk_mul_f32 v[88:89], v[88:89], v[218:219] op_sel_hi:[1,0]\n"
    "v_pk_mul_f32 v[90:91], v[90:91], v[218:219] op_sel_hi:[1,0]\n"
    "v_pk_mul_f32 v[92:93], v[92:93], v[218:219] op_sel_hi:[1,0]\n"
    "v_pk_mul_f32 v[94:95], v[94:95], v[218:219] op_sel_hi:[1,0]\n"
    "v_cvt_pk_bf16_f32 v64, v64, v65\n"
    "v_cvt_pk_bf16_f32 v65, v66, v67\n"
    "v_cvt_pk_bf16_f32 v66, v68, v69\n"
    "v_cvt_pk_bf16_f32 v67, v70, v71\n"
    "v_cvt_pk_bf16_f32 v72, v72, v73\n"
    "v_cvt_pk_bf16_f32 v73, v74, v75\n"
    "v_cvt_pk_bf16_f32 v74, v76, v77\n"
    "v_cvt_pk_bf16_f32 v75, v78, v79\n"
    "v_cvt_pk_bf16_f32 v80, v80, v81\n"
    "v_cvt_pk_bf16_f32 v81, v82, v83\n"
    "v_cvt_pk_bf16_f32 v82, v84, v85\n"
    "v_cvt_pk_bf16_f32 v83, v86, v87\n"
    "v_cvt_pk_bf16_f32 v88, v88, v89\n"
    "v_cvt_pk_bf16_f32 v89, v90, v91\n"
    "v_cvt_pk_bf16_f32 v90, v92, v93\n"
    "v_cvt_pk_bf16_f32 v91, v94, v95\n"
    "v_permlane16_swap_b32 v64, v66\n"
    "v_permlane16_swap_b32 v65, v67\n"
    "v_permlane16_swap_b32 v72, v74\n"
    "v_permlane16_swap_b32 v73, v75\n"
    "v_permlane16_swap_b32 v80, v82\n"
    "v_permlane16_swap_b32 v81, v83\n"
    "v_permlane16_swap_b32 v88, v90\n"
    "v_permlane16_swap_b32 v89, v91\n"
    "ds_write_b128 v226, v[64:67] offset:8192\n"
    "ds_write_b128 v227, v[72:75] offset:8192\n"
    "ds_write_b128 v226, v[80:83] offset:8320\n"
    "ds_write_b128 v227, v[88:91] offset:8320\n"
    "ds_read_b128 v[68:71], v228 offset:8192\n"
    "ds_read_b128 v[76:79], v229 offset:9216\n"
    "ds_read_b128 v[84:87], v228 offset:10240\n"
    "ds_read_b128 v[92:95], v229 offset:11264\n"
    "s_waitcnt lgkmcnt(3)\n"
    "buffer_store_dwordx4 v[68:71], v212, s[52:55], s68 offen sc0 sc1 nt\n"
    "s_waitcnt lgkmcnt(2)\n"
    "buffer_store_dwordx4 v[76:79], v212, s[52:55], s68 offen offset:1024 sc0 sc1 nt\n"
    "s_waitcnt lgkmcnt(1)\n"
    "buffer_store_dwordx4 v[84:87], v212, s[52:55], s68 offen offset:2048 sc0 sc1 nt\n"
    "s_waitcnt lgkmcnt(0)\n"
    "buffer_store_dwordx4 v[92:95], v212, s[52:55], s68 offen offset:3072 sc0 sc1 nt\n"
    "v_rcp_f32 v220, v204\n"
    "s_nop 0\n"
    "v_pk_mul_f32 v[96:97], v[96:97], v[220:221] op_sel_hi:[1,0]\n"
    "v_pk_mul_f32 v[98:99], v[98:99], v[220:221] op_sel_hi:[1,0]\n"
    "v_pk_mul_f32 v[100:101], v[100:101], v[220:221] op_sel_hi:[1,0]\n"
    "v_pk_mul_f32 v[102:103], v[102:103], v[220:221] op_sel_hi:[1,0]\n"
    "v_pk_mul_f32 v[104:105], v[104:105], v[220:221] op_sel_hi:[1,0]\n"
    "v_pk_mul_f32 v[106:107], v[106:107], v[220:221] op_sel_hi:[1,0]\n"
    "v_pk_mul_f32 v[108:109], v[108:109], v[220:221] op_sel_hi:[1,0]\n"
    "v_pk_mul_f32 v[110:111], v[110:111], v[220:221] op_sel_hi:[1,0]\n"
    "v_pk_mul_f32 v[112:113], v[112:113], v[220:221] op_sel_hi:[1,0]\n"
    "v_pk_mul_f32 v[114:115], v[114:115], v[220:221] op_sel_hi:[1,0]\n"
    "v_pk_mul_f32 v[116:117], v[116:117], v[220:221] op_sel_hi:[1,0]\n"
    "v_pk_mul_f32 v[118:119], v[118:119], v[220:221] op_sel_hi:[1,0]\n"
    "v_pk_mul_f32 v[120:121], v[120:121], v[220:221] op_sel_hi:[1,0]\n"
    "v_pk_mul_f32 v[122:123], v[122:123], v[220:221] op_sel_hi:[1,0]\n"
    "v_pk_mul_f32 v[124:125], v[124:125], v[220:221] op_sel_hi:[1,0]\n"
    "v_pk_mul_f32 v[126:127], v[126:127], v[220:221] op_sel_hi:[1,0]\n"
    "v_cvt_pk_bf16_f32 v96, v96, v97\n"
    "v_cvt_pk_bf16_f32 v97, v98, v99\n"
    "v_cvt_pk_bf16_f32 v98, v100, v101\n"
    "v_cvt_pk_bf16_f32 v99, v102, v103\n"
    "v_cvt_pk_bf16_f32 v104, v104, v105\n"
    "v_cvt_pk_bf16_f32 v105, v106, v107\n"
    "v_cvt_pk_bf16_f32 v106, v108, v109\n"
    "v_cvt_pk_bf16_f32 v107, v110, v111\n"
    "v_cvt_pk_bf16_f32 v112, v112, v113\n"
    "v_cvt_pk_bf16_f32 v113, v114, v115\n"
    "v_cvt_pk_bf16_f32 v114, v116, v117\n"
    "v_cvt_pk_bf16_f32 v115, v118, v119\n"
    "v_cvt_pk_bf16_f32 v120, v120, v121\n"
    "v_cvt_pk_bf16_f32 v121, v122, v123\n"
    "v_cvt_pk_bf16_f32 v122, v124, v125\n"
    "v_cvt_pk_bf16_f32 v123, v126, v127\n"
    "v_permlane16_swap_b32 v96, v98\n"
    "v_permlane16_swap_b32 v97, v99\n"
    "v_permlane16_swap_b32 v104, v106\n"
    "v_permlane16_swap_b32 v105, v107\n"
    "v_permlane16_swap_b32 v112, v114\n"
    "v_permlane16_swap_b32 v113, v115\n"
    "v_permlane16_swap_b32 v120, v122\n"
    "v_permlane16_swap_b32 v121, v123\n"
    "ds_write_b128 v226, v[96:99] offset:12288\n"
    "ds_write_b128 v227, v[104:107] offset:12288\n"
    "ds_write_b128 v226, v[112:115] offset:12416\n"
    "ds_write_b128 v227, v[120:123] offset:12416\n"
    "ds_read_b128 v[100:103], v228 offset:12288\n"
    "ds_read_b128 v[108:111], v229 offset:13312\n"
    "ds_read_b128 v[116:119], v228 offset:14336\n"
    "ds_read_b128 v[124:127], v229 offset:15360\n"
    "s_waitcnt lgkmcnt(3)\n"
    "buffer_store_dwordx4 v[100:103], v212, s[52:55], s69 offen sc0 sc1 nt\n"
    "s_waitcnt lgkmcnt(2)\n"
    "buffer_store_dwordx4 v[108:111], v212, s[52:55], s69 offen offset:1024 sc0 sc1 nt\n"
    "s_waitcnt lgkmcnt(1)\n"
    "buffer_store_dwordx4 v[116:119], v212, s[52:55], s69 offen offset:2048 sc0 sc1 nt\n"
    "s_waitcnt lgkmcnt(0)\n"
    "buffer_store_dwordx4 v[124:127], v212, s[52:55], s69 offen offset:3072 sc0 sc1 nt\n"
    "v_add_f32 v222, v192, v196\n"
    "v_add_f32 v223, v200, v204\n"
    "v_add_f32 v222, v222, v223\n"
    "s_mov_b32 s58, 0x5d800000\n"
    "v_cmp_nge_f32 s[74:75], s58, v222\n"
    "s_nop 3\n"
    "s_or_b32 s74, s74, s75\n"
    "s_cmp_lg_u32 s74, 0\n"
    "s_cbranch_scc1 Lslow\n"
    "s_endpgm\n"
    "Lslow:\n"
    :
    : "s"(0u), "s"(__builtin_amdgcn_kernarg_segment_ptr()), "s"(0u), "s"(0u), "s"(0u), "s"(head), "s"(wid)
    : "v0", "v1", "v2", "v3", "v4", "v5", "v6", "v7", "v8", "v9", "v10", "v11", "v12", "v13", "v14", "v15", "v16", "v17", "v18", "v19", "v20", "v21", "v22", "v23", "v24", "v25", "v26", "v27", "v28", "v29", "v30", "v31", "v32", "v33", "v34", "v35", "v36", "v37", "v38", "v39", "v40", "v41", "v42", "v43", "v44", "v45", "v46", "v47", "v48", "v49", "v50", "v51", "v52", "v53", "v54", "v55", "v56", "v57", "v58", "v59", "v60", "v61", "v62", "v63", "v64", "v65", "v66", "v67", "v68", "v69", "v70", "v71", "v72", "v73", "v74", "v75", "v76", "v77", "v78", "v79", "v80", "v81", "v82", "v83", "v84", "v85", "v86", "v87", "v88", "v89", "v90", "v91", "v92", "v93", "v94", "v95", "v96", "v97", "v98", "v99", "v100", "v101", "v102", "v103", "v104", "v105", "v106", "v107", "v108", "v109", "v110", "v111", "v112", "v113", "v114", "v115", "v116", "v117", "v118", "v119", "v120", "v121", "v122", "v123", "v124", "v125", "v126", "v127", "v128", "v129", "v130", "v131", "v132", "v133", "v134", "v135", "v136", "v137", "v138", "v139", "v140", "v141", "v142", "v143", "v144", "v145", "v146", "v147", "v148", "v149", "v150", "v151", "v152", "v153", "v154", "v155", "v156", "v157", "v158", "v159", "v160", "v161", "v162", "v163", "v164", "v165", "v166", "v167", "v168", "v169", "v170", "v171", "v172", "v173", "v174", "v175", "v176", "v177", "v178", "v179", "v180", "v181", "v182", "v183", "v184", "v185", "v186", "v187", "v188", "v189", "v190", "v191", "v192", "v193", "v194", "v195", "v196", "v197", "v198", "v199", "v200", "v201", "v202", "v203", "v204", "v205", "v206", "v207", "v208", "v209", "v210", "v211", "v212", "v213", "v214", "v215", "v216", "v217", "v218", "v219", "v220", "v221", "v222", "v223", "v224", "v225", "v226", "v227", "v228", "v229", "v230", "v231", "v232", "v233", "v234", "v235", "v236", "v237", "v238", "v239", "v240", "v241", "v242", "v243", "v244", "v245", "v246", "v247", "v248", "v249", "v250", "v251", "v252", "v253", "v254", "v255", "a0", "a1", "a2", "a3", "a4", "a5", "a6", "a7", "a8", "a9", "a10", "a11", "a12", "a13", "a14", "a15", "a16", "a17", "a18", "a19", "a20", "a21", "a22", "a23", "a24", "a25", "a26", "a27", "a28", "a29", "a30", "a31", "a32", "a33", "a34", "a35", "a36", "a37", "a38", "a39", "a40", "a41", "a42", "a43", "a44", "a45", "a46", "a47", "a48", "a49", "a50", "a51", "a52", "a53", "a54", "a55", "a56", "a57", "a58", "a59", "a60", "a61", "a62", "a63", "a64", "a65", "a66", "a67", "a68", "a69", "a70", "a71", "a72", "a73", "a74", "a75", "a76", "a77", "a78", "a79", "a80", "a81", "a82", "a83", "a84", "a85", "a86", "a87", "a88", "a89", "a90", "a91", "a92", "a93", "a94", "a95", "a96", "a97", "a98", "a99", "a100", "a101", "a102", "a103", "a104", "a105", "a106", "a107", "a108", "a109", "a110", "a111", "a112", "a113", "a114", "a115", "a116", "a117", "a118", "a119", "a120", "a121", "a122", "a123", "a124", "a125", "a126", "a127", "a128", "a129", "a130", "a131", "a132", "a133", "a134", "a135", "a136", "a137", "a138", "a139", "a140", "a141", "a142", "a143", "a144", "a145", "a146", "a147", "a148", "a149", "a150", "a151", "a152", "a153", "a154", "a155", "a156", "a157", "a158", "a159", "a160", "a161", "a162", "a163", "s40", "s41", "s42", "s43", "s44", "s45", "s46", "s47", "s48", "s49", "s50", "s51", "s52", "s53", "s54", "s55", "s56", "s57", "s58", "s59", "s60", "s61", "s62", "s63", "s64", "s65", "s66", "s67", "s68", "s69", "s70", "s71", "s72", "s73", "s74", "s75", "s76", "s77", "s78", "s79", "s80", "s81", "s82", "s83", "s84", "s85", "s86", "s87", "s88", "s89", "s90", "s91", "s92", "s93", "s94", "s95", "s96", "s97", "s98", "s99", "vcc", "scc", "memory");
  asm volatile("s_waitcnt vmcnt(0)" ::: "memory");
  slow_rows(Q, K, V, O, head, wid);
}

extern "C" void kernel_launch(void* const* d_in, const int* in_sizes, int n_in, void* d_out, int out_size, void* d_ws, size_t ws_size, hipStream_t stream) {
  hipLaunchKernelGGL(attn_fwd, dim3(256), dim3(256), 0, stream, (const char*)d_in[0], (const char*)d_in[1], (const char*)d_in[2], (char*)d_out);
}
```
